```python
import torch

def _softmax(x, axis=-1):
    return torch.softmax(x, dim=axis)

def _gelu(x):
    return torch.nn.functional.gelu(x, approximate='tanh')

def _silu(x):
    return torch.nn.functional.silu(x)

def _sigmoid(x):
    return torch.sigmoid(x)

def _layernorm(x, axis=-1, eps=1e-05):
    m = x.mean(dim=axis, keepdim=True)
    v = x.var(dim=axis, keepdim=True, unbiased=False)
    return (x - m) / torch.sqrt(v + eps)

def _scatter_add(tgt, idx, src, axis=0):
    return tgt.clone().index_add_(axis, idx, src)

def _segment_sum(src, seg, n):
    out = torch.zeros((n,) + src.shape[1:], dtype=src.dtype)
    return out.index_add_(0, seg, src)

def _pad(x, pw):
    flat = tuple((v for pair in reversed(pw) for v in pair))
    return torch.nn.functional.pad(x, flat)
kernel_name = 'adaLN_cond__sliding_block_cached__swiglu_ffn__decode'

def setup_inputs():

    _w = lambda a: (lambda t: t.to(torch.bfloat16) if t.is_floating_point() else t)(torch.as_tensor(a))

    def _A_setup():
        B, Sq, D = (1, 1, 1024)
        Hq, Hkv, Sk, Dh = (32, 4, 65536, 128)
        assert Hq % Hkv == 0, (Hq, Hkv)
        return {'x': _w((lambda *s: torch.randn(*s, dtype=torch.bfloat16) * 0.1)(B, Sq, D)), 'k_cache': _w((lambda *s: torch.randn(*s, dtype=torch.bfloat16) * 0.8)(B, Hkv, Sk, Dh)), 'v_cache': _w((lambda *s: torch.randn(*s, dtype=torch.bfloat16) * 0.8)(B, Hkv, Sk, Dh)), 'wq': _w((lambda *s: torch.randn(*s, dtype=torch.bfloat16) * 0.8)(D, Hq * Dh) * D ** (-0.5)), 'wo': _w((lambda *s: torch.randn(*s, dtype=torch.bfloat16) * 0.8)(Hq * Dh, D) * (Hq * Dh) ** (-0.5))}
    _A_in = _A_setup()

    def _B_setup():
        B, S, D, F = (1, 1, 1024, 3584)
        return {'x': _w((lambda *s: torch.randn(*s, dtype=torch.bfloat16) * 0.1)(B, S, D)), 'w_gate': _w((lambda *s: torch.randn(*s, dtype=torch.bfloat16) * 0.1)(D, F)), 'w_up': _w((lambda *s: torch.randn(*s, dtype=torch.bfloat16) * 0.1)(D, F)), 'w_down': _w((lambda *s: torch.randn(*s, dtype=torch.bfloat16) * 0.1)(F, D))}
    _B_in = _B_setup()
    return {'A_x': _A_in['x'], 'A_k_cache': _A_in['k_cache'], 'A_v_cache': _A_in['v_cache'], 'A_wq': _A_in['wq'], 'A_wo': _A_in['wo'], 'B_x': _B_in['x'], 'B_w_gate': _B_in['w_gate'], 'B_w_up': _B_in['w_up'], 'B_w_down': _B_in['w_down'], 'c': _w((lambda *s: torch.randn(*s, dtype=torch.bfloat16) * 0.2)(1, 1024) * 8.0), 'w_mod': _w((lambda *s: torch.randn(*s, dtype=torch.bfloat16) * 0.2)(1024, 6 * 1024) * 1024 ** (-0.5) * 8.0)}

def reference(A_x, A_k_cache, A_v_cache, A_wq, A_wo, B_x, B_w_gate, B_w_up, B_w_down, c, w_mod):

    def _A_ref(x, k_cache, v_cache, wq, wo):
        B, Sq, D = x.shape
        Hq, Dh = (32, 128)
        Hkv, Sk = (k_cache.shape[1], k_cache.shape[2])
        W = min(4096, max(256, Sk // 4))
        rep = Hq // Hkv
        q = (lambda x, *d: x.permute(*d))((x @ wq).reshape(B, Sq, Hq, Dh), 0, 2, 1, 3)
        k = (lambda x, n, axis: torch.repeat_interleave(x, n, dim=axis))(k_cache[:, :, Sk - W:, :], rep, axis=1)
        v = (lambda x, n, axis: torch.repeat_interleave(x, n, dim=axis))(v_cache[:, :, Sk - W:, :], rep, axis=1)
        scores = torch.einsum('bhqd,bhkd->bhqk', q, k) * Dh ** (-0.5)
        p = _softmax(scores, axis=-1)
        o = torch.einsum('bhqk,bhkd->bhqd', p, v)
        return (lambda x, *d: x.permute(*d))(o, 0, 2, 1, 3).reshape(B, Sq, Hq * Dh) @ wo

    def _B_ref(x, w_gate, w_up, w_down):
        g = _silu(x @ w_gate)
        u = x @ w_up
        return g * u @ w_down
    x = A_x
    mod = (_silu(c) @ w_mod).reshape(c.shape[0], 6, 1, -1)
    sa, ca, ga, sb, cb, gb = (mod[:, 0].clone(), mod[:, 1].clone(), mod[:, 2].clone(), mod[:, 3].clone(), mod[:, 4].clone(), mod[:, 5].clone())
    h = _layernorm(x, axis=-1) * (1 + ca) + sa
    x = x + ga * _A_ref(h, A_k_cache, A_v_cache, A_wq, A_wo)
    h = _layernorm(x, axis=-1) * (1 + cb) + sb
    x = x + gb * _B_ref(h, B_w_gate, B_w_up, B_w_down)
    return x
```

```cpp
#include <hip/hip_runtime.h>
#include <hip/hip_bf16.h>
typedef unsigned short u16;
typedef unsigned v4u __attribute__((ext_vector_type(4)));
__device__ __forceinline__ float bf2f(unsigned b) { return __uint_as_float(b << 16); }
__device__ __forceinline__ float rbf(float f) { return f; }
__device__ __forceinline__ float ldbf(const u16* p, int i) { return bf2f(p[i]); }
__device__ __forceinline__ float silu(float x) { return x / (1.f + __expf(-x)); }

constexpr int D = 1024, HQ = 32, HKV = 4, SK = 65536, DH = 128, W = 4096, F = 3584;

__device__ __forceinline__ void gemv_tile(const u16* Wm, int ldw, int col0, int k0, int KS, const float* xs, float* red, float* outp) {
  const int tid = threadIdx.x, c8 = tid & 7, r = tid >> 3;
  float acc[8];
#pragma unroll
  for (int j = 0; j < 8; ++j) acc[j] = 0.f;
  for (int k = r; k < KS; k += 32) {
    v4u w = *(const v4u*)(Wm + (size_t)(k0 + k) * ldw + col0 + c8 * 8);
    float xv = xs[k];
#pragma unroll
    for (int j = 0; j < 4; ++j) { acc[2 * j] += xv * bf2f(w[j] & 0xffffu); acc[2 * j + 1] += xv * __uint_as_float(w[j] & 0xffff0000u); }
  }
#pragma unroll
  for (int j = 0; j < 8; ++j) red[r * 64 + c8 * 8 + j] = acc[j];
  __syncthreads();
  if (tid < 64) { float s = 0.f; for (int i = 0; i < 32; ++i) s += red[i * 64 + tid]; outp[tid] = s; }
  __syncthreads();
}
__device__ __forceinline__ float block_sum(float v, float* sm) {
  for (int o = 32; o; o >>= 1) v += __shfl_xor(v, o);
  __syncthreads();
  if ((threadIdx.x & 63) == 0) sm[threadIdx.x >> 6] = v;
  __syncthreads();
  return sm[0] + sm[1] + sm[2] + sm[3];
}
__device__ __forceinline__ void ln_mod(const float* xv  , const float* sh, const float* sc, float* hs, float* sm) {
  float s = xv[0] + xv[1] + xv[2] + xv[3];
  float mean = block_sum(s, sm) * (1.f / D);
  float q = 0.f;
  for (int j = 0; j < 4; ++j) { float d = xv[j] - mean; q += d * d; }
  float var = rbf(block_sum(q, sm) * (1.f / D));
  float mb = rbf(mean);
  float sd = rbf(sqrtf(rbf(var + 1e-5f)));
  for (int j = 0; j < 4; ++j) { int i = threadIdx.x * 4 + j; float n = rbf(rbf(xv[j] - mb) / sd); hs[i] = rbf(rbf(n * rbf(1.f + sc[j])) + sh[j]); }
  __syncthreads();
}

__global__ void __launch_bounds__(256) k_mod(const u16* c, const u16* wm, float* modp) {
  __shared__ float xs[256], red[32 * 64];
  const int k0 = blockIdx.y * 256;
  xs[threadIdx.x] = rbf(silu(ldbf(c, k0 + threadIdx.x)));
  __syncthreads();
  gemv_tile(wm, 6 * D, blockIdx.x * 64, k0, 256, xs, red, modp + blockIdx.y * 6 * D + blockIdx.x * 64);
}
__device__ __forceinline__ float modv(const float* modp, int i) { return rbf(modp[i] + modp[6 * D + i] + modp[12 * D + i] + modp[18 * D + i]); }

__global__ void __launch_bounds__(256) k_q(const u16* x, const float* modp, const u16* wq, float* qp) {
  __shared__ float hs[D], red[32 * 64], sm[4];
  float xv[4], sh[4], sc[4];
  for (int j = 0; j < 4; ++j) { int i = threadIdx.x * 4 + j; xv[j] = ldbf(x, i); sh[j] = modv(modp, i); sc[j] = modv(modp, D + i); }
  ln_mod(xv, sh, sc, hs, sm);
  const int k0 = blockIdx.y * 256;
  gemv_tile(wq, HQ * DH, blockIdx.x * 64, k0, 256, hs + k0, red, qp + blockIdx.y * 4096 + blockIdx.x * 64);
}
__global__ void __launch_bounds__(256) k_attn(const float* qp, const u16* kc, const u16* vc, float* po, float* pml) {
  __shared__ float qs[8 * 128], sc[8 * 64], ml[16];
  const int g = blockIdx.y, ch = blockIdx.x, tid = threadIdx.x;
  for (int i = tid; i < 1024; i += 256) { int qi = g * 1024 + i; qs[i] = rbf(qp[qi] + qp[4096 + qi] + qp[8192 + qi] + qp[12288 + qi]); }
  __syncthreads();
  const size_t base = ((size_t)g * SK + (SK - W) + ch * 64) * DH;
  for (int t = tid; t < 512; t += 256) {
    int h = t >> 6, key = t & 63; float s = 0.f;
    for (int d = 0; d < 128; ++d) s += qs[h * 128 + d] * ldbf(kc, base + key * DH + d);
    sc[t] = rbf(rbf(s) * 0.08838834764831845f);
  }
  __syncthreads();
  if (tid < 8) { float m = -INFINITY; for (int k = 0; k < 64; ++k) m = fmaxf(m, sc[tid * 64 + k]); float l = 0.f; for (int k = 0; k < 64; ++k) { float p = __expf(sc[tid * 64 + k] - m); sc[tid * 64 + k] = p; l += p; } ml[tid * 2] = m; ml[tid * 2 + 1] = l; }
  __syncthreads();
  for (int t = tid; t < 1024; t += 256) {
    int h = t >> 7, d = t & 127; float o = 0.f;
    for (int k = 0; k < 64; ++k) o += sc[h * 64 + k] * ldbf(vc, base + k * DH + d);
    po[(size_t)(g * 64 + ch) * 1024 + t] = o;
  }
  if (tid < 16) pml[(g * 64 + ch) * 16 + tid] = ml[tid];
}
__global__ void __launch_bounds__(256) k_wo(const float* po, const float* pml, const u16* wo, float* ap) {
  __shared__ float os[256], red[32 * 64];
  const int k0 = blockIdx.y * 256, i = k0 + threadIdx.x, head = i >> 7, g = head >> 3, hh = head & 7, d = i & 127;
  float M = -INFINITY;
  for (int c = 0; c < 64; ++c) M = fmaxf(M, pml[(g * 64 + c) * 16 + hh * 2]);
  float L = 0.f, o = 0.f;
  for (int c = 0; c < 64; ++c) { float w = __expf(pml[(g * 64 + c) * 16 + hh * 2] - M); L += w * pml[(g * 64 + c) * 16 + hh * 2 + 1]; o += w * po[(size_t)(g * 64 + c) * 1024 + hh * 128 + d]; }
  os[threadIdx.x] = rbf(o / L);
  __syncthreads();
  gemv_tile(wo, D, blockIdx.x * 64, k0, 256, os, red, ap + blockIdx.y * D + blockIdx.x * 64);
}
__device__ __forceinline__ float x1v(const u16* x, const float* modp, const float* ap, int i) {
  float a = 0.f; for (int s = 0; s < 16; ++s) a += ap[s * D + i];
  return rbf(ldbf(x, i) + rbf(modv(modp, 2 * D + i) * rbf(a)));
}
__global__ void __launch_bounds__(256) k_gu(const u16* x, const float* modp, const float* ap, const u16* wg, const u16* wu, float* gup) {
  __shared__ float hs[D], red[32 * 64], sm[4];
  float xv[4], sh[4], sc[4];
  for (int j = 0; j < 4; ++j) { int i = threadIdx.x * 4 + j; xv[j] = x1v(x, modp, ap, i); sh[j] = modv(modp, 3 * D + i); sc[j] = modv(modp, 4 * D + i); }
  ln_mod(xv, sh, sc, hs, sm);
  const int k0 = blockIdx.y * 256, bx = blockIdx.x;
  const bool up = bx >= 56; const int cb = (up ? bx - 56 : bx) * 64;
  gemv_tile(up ? wu : wg, F, cb, k0, 256, hs + k0, red, gup + blockIdx.y * 2 * F + (up ? F : 0) + cb);
}
__global__ void __launch_bounds__(256) k_down(const float* gup, const u16* wd, float* yp) {
  __shared__ float as[256], red[32 * 64];
  const int k0 = blockIdx.y * 256, i = k0 + threadIdx.x;
  float g = rbf(gup[i] + gup[2 * F + i] + gup[4 * F + i] + gup[6 * F + i]);
  float u = rbf(gup[F + i] + gup[3 * F + i] + gup[5 * F + i] + gup[7 * F + i]);
  as[threadIdx.x] = rbf(rbf(silu(g)) * u);
  __syncthreads();
  gemv_tile(wd, D, blockIdx.x * 64, k0, 256, as, red, yp + blockIdx.y * D + blockIdx.x * 64);
}
__global__ void __launch_bounds__(256) k_fin(const u16* x, const float* modp, const float* ap, const float* yp, __hip_bfloat16* out) {
  int i = blockIdx.x * 256 + threadIdx.x;
  float y = 0.f; for (int s = 0; s < 14; ++s) y += yp[s * D + i];
  float r = x1v(x, modp, ap, i) + rbf(modv(modp, 5 * D + i) * rbf(y));
  out[i] = __float2bfloat16(r);
}

extern "C" void kernel_launch(void* const* d_in, const int* in_sizes, int n_in, void* d_out, int out_size, void* d_ws, size_t ws_size, hipStream_t stream) {
  const u16* x = (const u16*)d_in[0]; const u16* kc = (const u16*)d_in[1]; const u16* vc = (const u16*)d_in[2];
  const u16* wq = (const u16*)d_in[3]; const u16* wo = (const u16*)d_in[4];
  const u16* wg = (const u16*)d_in[6]; const u16* wu = (const u16*)d_in[7]; const u16* wd = (const u16*)d_in[8];
  const u16* c = (const u16*)d_in[9]; const u16* wm = (const u16*)d_in[10];
  float* ws = (float*)d_ws;
  float* modp = ws;
  float* qp = modp + 4 * 6144;
  float* po = qp + 4 * 4096;
  float* pml = po + 256 * 1024;
  float* ap = pml + 256 * 16;
  float* gup = ap + 16 * 1024;
  float* yp = gup + 8 * F;
  k_mod<<<dim3(96, 4), 256, 0, stream>>>(c, wm, modp);
  k_q<<<dim3(64, 4), 256, 0, stream>>>(x, modp, wq, qp);
  k_attn<<<dim3(64, 4), 256, 0, stream>>>(qp, kc, vc, po, pml);
  k_wo<<<dim3(16, 16), 256, 0, stream>>>(po, pml, wo, ap);
  k_gu<<<dim3(112, 4), 256, 0, stream>>>(x, modp, ap, wg, wu, gup);
  k_down<<<dim3(16, 14), 256, 0, stream>>>(gup, wd, yp);
  k_fin<<<dim3(4), 256, 0, stream>>>(x, modp, ap, yp, (__hip_bfloat16*)d_out);
}
```

```cpp
#include <hip/hip_runtime.h>
#include <hip/hip_bf16.h>

#define PROF 0

namespace {
typedef unsigned short u16;
typedef unsigned v4u __attribute__((ext_vector_type(4)));
typedef unsigned v2u __attribute__((ext_vector_type(2)));
typedef float f32x4 __attribute__((ext_vector_type(4)));
typedef __bf16 bf16x8 __attribute__((ext_vector_type(8)));
typedef __amdgpu_buffer_rsrc_t rsrc_t;
#define LAS __attribute__((address_space(3)))
#define GAS __attribute__((address_space(1)))

constexpr int D = 1024, SK = 65536, WIN = 4096, FF = 3584;
constexpr int AUX_NT = 2, AUX_SC1 = 16;
constexpr unsigned SPIN_CAP = 1u << 17;

constexpr int WS_EPOCH = 0;
constexpr int WS_H1 = 4096;
constexpr int WS_H2 = WS_H1 + 32768;
constexpr int WS_H3S = WS_H2 + 65536;
constexpr int WS_HA2 = WS_H3S + 16384;
constexpr int WS_H4B = WS_HA2 + 131072;
constexpr int WS_H5 = WS_H4B + 16384;
constexpr int WS_H3D = WS_H5 + 114688;
constexpr int WS_H4A = WS_H3D + 524288;
constexpr int WS_H6 = WS_H4A + 524288;
constexpr int WS_END = WS_H6 + 458752;
constexpr int WS_PROF = 4 << 20;

constexpr int L_V = 0;
constexpr int L_RED0 = 32768;
constexpr int L_RED1 = L_RED0 + 8192;
constexpr int L_RED2 = L_RED1 + 8192;
constexpr int L_Q = L_RED2 + 8192;
constexpr int L_P = L_Q + 8 * 272;
constexpr int L_WMAX = L_P + 8 * 272;
constexpr int L_WSUM = L_WMAX + 256;
constexpr int L_O = L_WSUM + 256;
constexpr int L_VEC = L_O + 128;
constexpr int L_R1 = L_VEC + 512;
constexpr int L_R2 = L_R1 + 1024;
constexpr int LDS_BYTES = 84 * 1024;
static_assert(L_R2 + 1024 <= LDS_BYTES, "lds");

struct Args {
  const u16 *x, *kc, *vc, *wq, *wo, *wg, *wu, *wd, *c, *wm;
  u16* out;
  unsigned char* ws;
};

__device__ __forceinline__ rsrc_t mk(const void* p, unsigned bytes) { return __builtin_amdgcn_make_buffer_rsrc((void*)p, 0, bytes, 0x00020000); }
__device__ __forceinline__ v4u ldw(rsrc_t r, int voff, int soff) { return __builtin_amdgcn_raw_buffer_load_b128(r, voff, soff, AUX_NT); }
__device__ __forceinline__ v4u ldh(rsrc_t r, int voff) { return __builtin_amdgcn_raw_buffer_load_b128(r, voff, 0, AUX_SC1); }
__device__ __forceinline__ void sth(rsrc_t r, int voff, v4u v) { __builtin_amdgcn_raw_buffer_store_b128(v, r, voff, 0, AUX_SC1); }
__device__ __forceinline__ void sth2(rsrc_t r, int voff, v2u v) { __builtin_amdgcn_raw_buffer_store_b64(v, r, voff, 0, AUX_SC1); }
__device__ __forceinline__ float asf(unsigned u) { return __builtin_bit_cast(float, u); }
__device__ __forceinline__ unsigned asu(float f) { return __builtin_bit_cast(unsigned, f); }
__device__ __forceinline__ float rbf(float f) { return (float)(__bf16)f; }
__device__ __forceinline__ unsigned bfbits(float f) { return (unsigned)__builtin_bit_cast(u16, (__bf16)f); }
__device__ __forceinline__ unsigned dup16(float f) { const unsigned u = bfbits(f); return u | (u << 16); }
__device__ __forceinline__ float silu(float x) { return x * __builtin_amdgcn_rcpf(1.f + __expf(-x)); }
template <int C> __device__ __forceinline__ unsigned dppu(unsigned x) { return (unsigned)__builtin_amdgcn_mov_dpp((int)x, C, 0xf, 0xf, true); }
template <int C> __device__ __forceinline__ float dpp(float x) { return asf(dppu<C>(asu(x))); }
constexpr int XOR1 = 0xB1, XOR2 = 0x4E, HMIR = 0x141, ROR8 = 0x128, BCAST = 0x150;
struct F2 { float a, b; };
__device__ __forceinline__ F2 sw32(float a, float b) { auto r = __builtin_amdgcn_permlane32_swap(asu(a), asu(b), false, false); return {asf(r[0]), asf(r[1])}; }
__device__ __forceinline__ F2 sw16(float a, float b) { auto r = __builtin_amdgcn_permlane16_swap(asu(a), asu(b), false, false); return {asf(r[0]), asf(r[1])}; }
__device__ __forceinline__ float tr32(float a, float b) { F2 r = sw32(a, b); return r.a + r.b; }
__device__ __forceinline__ float tr16(float a, float b) { F2 r = sw16(a, b); return r.a + r.b; }
__device__ __forceinline__ float sum8(float v) { v += dpp<XOR1>(v); v += dpp<XOR2>(v); v += dpp<HMIR>(v); return v; }
__device__ __forceinline__ float sum16(float v) { v = sum8(v); v += dpp<ROR8>(v); return v; }
__device__ __forceinline__ float sum32(float v) { v = sum16(v); return tr16(v, v); }
__device__ __forceinline__ float sum64(float v) { v = sum32(v); return tr32(v, v); }
__device__ __forceinline__ float max32(float v) { v = fmaxf(v, dpp<XOR1>(v)); v = fmaxf(v, dpp<XOR2>(v)); v = fmaxf(v, dpp<HMIR>(v)); v = fmaxf(v, dpp<ROR8>(v)); F2 r = sw16(v, v); return fmaxf(r.a, r.b); }
__device__ __forceinline__ f32x4 mfma(v4u a, v4u b, f32x4 c) { return __builtin_amdgcn_mfma_f32_16x16x32_bf16(__builtin_bit_cast(bf16x8, a), __builtin_bit_cast(bf16x8, b), c, 0, 0, 0); }
__device__ __forceinline__ void bar() { asm volatile("s_waitcnt lgkmcnt(0)" ::: "memory"); __builtin_amdgcn_s_barrier(); asm volatile("" ::: "memory"); }
#define SB() __builtin_amdgcn_sched_barrier(0)

__device__ __forceinline__ v2u pack2(float a, float b, unsigned tag) {
  const unsigned ua = asu(a) + 0x200u, ub = asu(b) + 0x200u;
  v2u r;
  r.x = ((ub >> 10) & 0x3FFFFFu) | ((ua << 12) & 0xFFC00000u);
  r.y = __builtin_amdgcn_alignbit(tag, ua, 20);
  return r;
}
__device__ __forceinline__ v4u pack4(f32x4 v, unsigned tag) { const v2u p = pack2(v[0], v[1], tag), q = pack2(v[2], v[3], tag); return v4u{p.x, p.y, q.x, q.y}; }
__device__ __forceinline__ float upa(unsigned lo, unsigned hi) { return asf(__builtin_amdgcn_alignbit(hi, lo, 12) & 0xFFFFFC00u); }
__device__ __forceinline__ float upb(unsigned lo) { return asf(lo << 10); }
__device__ __forceinline__ f32x4 unpack4(v4u u) { return f32x4{upa(u.x, u.y), upb(u.x), upa(u.z, u.w), upb(u.z)}; }
__device__ __forceinline__ bool tagok(v4u u, unsigned tsh) { return ((u.y ^ tsh) | (u.w ^ tsh)) < 4096u; }

template <int N> __device__ __forceinline__ void gather(rsrc_t ws, const int (&off)[N], v4u (&u)[N], unsigned tsh) {
  for (unsigned spins = 0;; ++spins) {
    asm volatile("" ::: "memory");
#pragma unroll
    for (int i = 0; i < N; ++i) u[i] = ldh(ws, off[i]);
    bool ok = true;
#pragma unroll
    for (int i = 0; i < N; ++i) ok &= tagok(u[i], tsh);
    if (__all(ok) || spins > SPIN_CAP) break;
    __builtin_amdgcn_s_sleep(1);
  }
  asm volatile("" ::: "memory");
}
template <int M> __device__ __forceinline__ v4u diag(unsigned xx, v4u mask) { const unsigned t = dppu<BCAST + M>(xx); return v4u{t & mask.x, t & mask.y, t & mask.z, t & mask.w}; }
__device__ __forceinline__ F2 red_pair(f32x4 v) {
  const F2 p = sw32(v[0], v[2]), q = sw32(v[1], v[3]);
  float s0 = tr16(p.a, q.a), s1 = tr16(p.b, q.b);
  s0 += dpp<ROR8>(s0); s1 += dpp<ROR8>(s1);
  return {s0, s1};
}
__device__ __forceinline__ float red8(f32x4 v) {
  float s = tr16(tr32(v[0], v[2]), tr32(v[1], v[3]));
  s += dpp<ROR8>(s);
  return s;
}
template <class T> __device__ __forceinline__ LAS T* lp(LAS unsigned char* L, int off) { return (LAS T*)(L + off); }

#define STAMP(i) do {} while (0)

__device__ __forceinline__ void final32(LAS unsigned char* L, int red, int pitch_units, int u0, rsrc_t ws, int dst, int lane, unsigned tag) {
  const int u = lane & 31, hf = lane >> 5;
  f32x4 s = lp<f32x4>(L, red)[(4 * hf) * pitch_units + u0 + u];
#pragma unroll
  for (int i = 1; i < 4; ++i) s += lp<f32x4>(L, red)[(4 * hf + i) * pitch_units + u0 + u];
  const float e0 = tr32(s[0], s[2]), e1 = tr32(s[1], s[3]);
  sth2(ws, dst + u * 16 + hf * 8, pack2(e0, e1, tag));
}

__global__ void __launch_bounds__(512, 2) fused_block(Args a) {
  __shared__ __attribute__((aligned(16))) unsigned char smem[LDS_BYTES];
  LAS unsigned char* L = (LAS unsigned char*)smem;
  const int b = blockIdx.x, tid = threadIdx.x, lane = tid & 63, w = __builtin_amdgcn_readfirstlane(tid >> 6);
  const int xcd = b & 7, slot = b >> 3, j = slot >> 1;
  const bool isY = slot & 1;
  const int c16 = lane & 15, h = lane >> 4, c8 = lane & 7;
  const rsrc_t ws = mk(a.ws, WS_END), rwm = mk(a.wm, D * 6144 * 2), rc = mk(a.c, D * 2), rx = mk(a.x, D * 2);
  const unsigned ep = __builtin_amdgcn_raw_buffer_load_b32(ws, WS_EPOCH + 4, 0, AUX_SC1);
  v4u pf = {0, 0, 0, 0};
  if (w == 7) {
    unsigned long long pc; asm volatile("s_getpc_b64 %0" : "=s"(pc));
    pf = *(const GAS v4u*)((pc & ~255ull) + (slot & 15) * 1024 + lane * 16);
  }
  v4u dmask, dmask2;
  {
    const unsigned mv = (c16 & 1) ? 0xFFFF0000u : 0x0000FFFFu;
    const int t = c8 >> 1;
    dmask2 = v4u{t == 0 ? mv : 0u, t == 1 ? mv : 0u, t == 2 ? mv : 0u, t == 3 ? mv : 0u};
    dmask = c16 < 8 ? dmask2 : v4u{0, 0, 0, 0};
  }
  const int P = xcd >> 1, pj = (xcd & 1) * 16 + j;
  const int cg32 = xcd + 8 * (j & 3), ks4 = j >> 2;
  const bool isR = j >= 14;
  unsigned tag, tsh;

  auto a2_issue = [&](v4u (&wv)[4], v2u& cv) {
    const int r0 = (isY ? 0 : 512) + 128 * ks4 + 16 * w + 4 * h;
    cv = __builtin_amdgcn_raw_buffer_load_b64(rc, r0 * 2, 0, 0);
    const int vo = r0 * 12288 + 4096 + cg32 * 256 + c16 * 16;
#pragma unroll
    for (int m = 0; m < 4; ++m) wv[m] = ldw(rwm, vo, m * 12288);
  };
  auto sdiag = [&](v2u cv, v4u mask, v4u (&A)[4]) {
    const float s0 = silu(asf(cv.x << 16)), s1 = silu(asf(cv.x & 0xFFFF0000u)), s2 = silu(asf(cv.y << 16)), s3 = silu(asf(cv.y & 0xFFFF0000u));
    const unsigned d[4] = {dup16(s0), dup16(s1), dup16(s2), dup16(s3)};
#pragma unroll
    for (int m = 0; m < 4; ++m) A[m] = v4u{d[m] & mask.x, d[m] & mask.y, d[m] & mask.z, d[m] & mask.w};
  };
  auto a2_finish = [&](v4u (&wv)[4], v2u cv, int red) {
    v4u A[4]; sdiag(cv, dmask, A);
    f32x4 acc = {0, 0, 0, 0};
#pragma unroll
    for (int m = 0; m < 4; ++m) acc = mfma(A[m], wv[m], acc);
    if (lane < 32) lp<f32x4>(L, red)[w * 32 + 2 * c16 + h] = acc;
    bar();
    if (w == 0) final32(L, red, 32, 0, ws, WS_HA2 + (((isY ? 0 : 4) + ks4) * 32 + cg32) * 512, lane, tag);
  };

  if (!isY) {
    {
      const int r0 = 256 * ks4 + 32 * w + 4 * (2 * h + (c16 >> 3));
      const v2u cv = __builtin_amdgcn_raw_buffer_load_b64(rc, r0 * 2, 0, 0);
      v4u wv[4];
      const int vo = r0 * 12288 + cg32 * 128 + c8 * 16;
#pragma unroll
      for (int m = 0; m < 4; ++m) wv[m] = ldw(rwm, vo, m * 12288);
      SB();
      tag = (__builtin_amdgcn_readfirstlane(ep) >> 12) + 1u & 0xFFFFFu; tsh = tag << 12;
      v4u A[4]; sdiag(cv, dmask2, A);
      asm volatile("" :: "v"(pf));
      f32x4 acc = {0, 0, 0, 0};
#pragma unroll
      for (int m = 0; m < 4; ++m) acc = mfma(A[m], wv[m], acc);
      STAMP(1);
      if ((c16 >> 3) == (h >> 1)) lp<f32x4>(L, L_RED0)[(w * 2 + (h >> 1)) * 16 + 2 * c8 + (h & 1)] = acc;
      bar();
      if (w == 0) {
        f32x4 s = lp<f32x4>(L, L_RED0)[(4 * h) * 16 + c16];
#pragma unroll
        for (int i = 1; i < 4; ++i) s += lp<f32x4>(L, L_RED0)[(4 * h + i) * 16 + c16];
        float e0 = tr32(s[0], s[2]), e1 = tr32(s[1], s[3]);
        e0 = tr16(e0, e0); e1 = tr16(e1, e1);
        if (!(h & 1)) sth2(ws, WS_H1 + (ks4 * 32 + cg32) * 256 + c16 * 16 + (h >> 1) * 8, pack2(e0, e1, tag));
      }
      STAMP(2);
    }
    const int g = P, chunk = pj;
    v4u kf[4], vf[4];
    {
      const rsrc_t rk = mk(a.kc + (size_t)(g * SK + SK - WIN + chunk * 128) * 128, 32768), rv = mk(a.vc + (size_t)(g * SK + SK - WIN + chunk * 128) * 128, 32768);
      v4u vv[4];
#pragma unroll
      for (int t = 0; t < 4; ++t) vv[t] = ldw(rv, w * 4096 + lane * 16, t * 1024);
#pragma unroll
      for (int s = 0; s < 4; ++s) kf[s] = ldw(rk, (16 * w + c16) * 256 + h * 16, s * 64);
      SB();
#pragma unroll
      for (int t = 0; t < 4; ++t) { const int row = 16 * w + 4 * t + h; *lp<v4u>(L, L_V + row * 256 + 16 * (c16 ^ ((h << 2) | t))) = vv[t]; }
      bar();
#pragma unroll
      for (int s = 0; s < 4; ++s) {
        v2u lo, hi;
        const int q = c16 >> 2, p = c16 & 3, r0 = 32 * s + 8 * h + q, ch = 2 * w + (p >> 1);
        const int a0 = L_V + r0 * 256 + 16 * (ch ^ ((q << 2) | ((2 * h) & 3))) + 8 * (p & 1);
        const int a1 = L_V + (r0 + 4) * 256 + 16 * (ch ^ ((q << 2) | ((2 * h + 1) & 3))) + 8 * (p & 1);
        const unsigned base = (unsigned)(size_t)L;
        asm volatile("ds_read_b64_tr_b16 %0, %2\n\tds_read_b64_tr_b16 %1, %3\n\ts_waitcnt lgkmcnt(0)" : "=&v"(lo), "=&v"(hi) : "v"(base + a0), "v"(base + a1) : "memory");
        vf[s] = v4u{lo.x, lo.y, hi.x, hi.y};
      }
      asm volatile("s_waitcnt vmcnt(0)" ::: "memory");
      STAMP(3);
    }
    {
      const int off[2] = {WS_H2 + ((g * 8 + w) * 4 + (lane >> 5)) * 512 + (lane & 31) * 16, WS_H2 + ((g * 8 + w) * 4 + 2 + (lane >> 5)) * 512 + (lane & 31) * 16};
      v4u u[2]; gather<2>(ws, off, u, tsh);
      STAMP(4);
      const f32x4 s = unpack4(u[0]) + unpack4(u[1]);
      const float e0 = tr32(s[0], s[2]), e1 = tr32(s[1], s[3]);
      *lp<unsigned>(L, L_Q + w * 272 + (lane & 31) * 8 + (lane >> 5) * 4) = bfbits(e0) | (bfbits(e1) << 16);
      bar();
      f32x4 sc = {0, 0, 0, 0};
#pragma unroll
      for (int s2 = 0; s2 < 4; ++s2) sc = mfma(kf[s2], *lp<v4u>(L, L_Q + c8 * 272 + s2 * 64 + h * 16), sc);
#pragma unroll
      for (int r = 0; r < 4; ++r) sc[r] = rbf(rbf(sc[r]) * 0.08838834764831845f);
      float mx = fmaxf(fmaxf(sc[0], sc[1]), fmaxf(sc[2], sc[3]));
      { F2 t = sw16(mx, mx); mx = fmaxf(t.a, t.b); t = sw32(mx, mx); mx = fmaxf(t.a, t.b); }
      if (lane < 8) lp<float>(L, L_WMAX)[lane * 8 + w] = mx;
      bar();
      const f32x4 m0 = lp<f32x4>(L, L_WMAX)[c8 * 2], m1 = lp<f32x4>(L, L_WMAX)[c8 * 2 + 1];
      const float m = fmaxf(fmaxf(fmaxf(m0[0], m0[1]), fmaxf(m0[2], m0[3])), fmaxf(fmaxf(m1[0], m1[1]), fmaxf(m1[2], m1[3])));
      float p[4];
#pragma unroll
      for (int r = 0; r < 4; ++r) p[r] = __builtin_amdgcn_exp2f((sc[r] - m) * 1.4426950408889634f);
      if (c16 < 8) *lp<v2u>(L, L_P + c8 * 272 + w * 32 + h * 8) = v2u{bfbits(p[0]) | (bfbits(p[1]) << 16), bfbits(p[2]) | (bfbits(p[3]) << 16)};
      float ls = (p[0] + p[1]) + (p[2] + p[3]);
      ls = tr16(ls, ls); ls = tr32(ls, ls);
      if (lane < 8) lp<float>(L, L_WSUM)[lane * 8 + w] = ls;
      bar();
      f32x4 o = {0, 0, 0, 0};
#pragma unroll
      for (int s2 = 0; s2 < 4; ++s2) o = mfma(vf[s2], *lp<v4u>(L, L_P + c8 * 272 + s2 * 64 + h * 16), o);
      if (w == 7) {
        const f32x4 l0 = lp<f32x4>(L, L_WSUM)[c8 * 2], l1 = lp<f32x4>(L, L_WSUM)[c8 * 2 + 1];
        const float l = ((l0[0] + l0[1]) + (l0[2] + l0[3])) + ((l1[0] + l1[1]) + (l1[2] + l1[3]));
        if (lane < 8) sth2(ws, WS_H3S + (g * 32 + chunk) * 128 + lane * 8, pack2(m, l, tag));
      }
      if (c16 < 8) sth(ws, WS_H3D + ((g * 32 + chunk) * 32 + w * 4 + (c16 >> 1)) * 128 + ((c16 & 1) * 4 + h) * 16, pack4(o, tag));
      STAMP(5);
    }
    v4u a2w[4]; v2u a2c;
    a2_issue(a2w, a2c);
    if (!isR) {
      const int e = xcd * 14 + j, cg = e % 28, ks = e / 28;
      const rsrc_t rg = mk(a.wg, D * FF * 2), ru = mk(a.wu, D * FF * 2);
      v4u gw[8], uw[8];
      const int vo = (256 * ks + 32 * w + h) * (FF * 2) + cg * 256 + c16 * 16;
#pragma unroll
      for (int m = 0; m < 8; ++m) { gw[m] = ldw(rg, vo, m * 4 * FF * 2); uw[m] = ldw(ru, vo, m * 4 * FF * 2); }
      SB();
      a2_finish(a2w, a2c, L_RED1);
      STAMP(6);
      asm volatile("s_waitcnt vmcnt(2)" ::: "memory");
      STAMP(7);
      const int rb = WS_H4B + (8 * ks + w) * 512 + c8 * 16;
      const int off[4] = {rb, WS_H4B + (lane & 31) * 512 + 128, rb + 256, rb + 384};
      v4u u[4]; gather<4>(ws, off, u, tsh);
      STAMP(8);
      const float mr = asf(u[1].x), m2r = asf(u[1].z);
      const float mean = sum32(mr) * (1.f / 32.f);
      const float dm = mr - mean;
      const float var = rbf(sum32(m2r + 32.f * dm * dm) * (1.f / D));
      const float sd = rbf(__builtin_sqrtf(rbf(var + 1e-5f)));
      auto sel = [&](v4u t) { const unsigned lo = (h & 2) ? t.z : t.x, hi = (h & 2) ? t.w : t.y; return (h & 1) ? upb(lo) : upa(lo, hi); };
      const float nrm = rbf(rbf(sel(u[0]) - rbf(mean)) / sd);
      const unsigned xx = dup16(rbf(nrm * sel(u[2])) + sel(u[3]));
      f32x4 ag = {0, 0, 0, 0}, au = {0, 0, 0, 0};
#define EST(M) { const v4u A = diag<M>(xx, dmask); ag = mfma(A, gw[M], ag); au = mfma(A, uw[M], au); }
      EST(0) EST(1) EST(2) EST(3) EST(4) EST(5) EST(6) EST(7)
#undef EST
      if (lane < 32) { lp<f32x4>(L, L_RED2)[w * 64 + 2 * c16 + h] = ag; lp<f32x4>(L, L_RED2)[w * 64 + 32 + 2 * c16 + h] = au; }
      bar();
      if (w < 2) {
        const int u5 = lane & 31;
        final32(L, L_RED2, 64, 32 * w, ws, WS_H5 + (ks * 28 + cg) * 1024 + (u5 >> 3) * 256 + w * 128 + (u5 & 7) * 16 - u5 * 16, lane, tag);
      }
      STAMP(9);
      return;
    }
    SB();
    a2_finish(a2w, a2c, L_RED1);
    STAMP(6);
  } else {
    const int g = (P + 3) & 3;
    {
      const int hd = pj >> 2, kb = pj & 3;
      const rsrc_t rq = mk(a.wq, D * 4096 * 2);
      const v4u x0 = __builtin_amdgcn_raw_buffer_load_b128(rx, lane * 32, 0, 0), x1 = __builtin_amdgcn_raw_buffer_load_b128(rx, lane * 32 + 16, 0, 0);
      const unsigned xe = __builtin_amdgcn_raw_buffer_load_b16(rx, (256 * kb + 32 * w + 4 * c8 + h) * 2, 0, 0);
      v4u wv[8];
      const int vo = (256 * kb + 32 * w + h) * 8192 + (g * 8 + hd) * 256 + c16 * 16;
#pragma unroll
      for (int m = 0; m < 8; ++m) wv[m] = ldw(rq, vo, m * 32768);
      SB();
      tag = (__builtin_amdgcn_readfirstlane(ep) >> 12) + 1u & 0xFFFFFu; tsh = tag << 12;
      asm volatile("" :: "v"(pf));
      float xv[16];
#pragma unroll
      for (int i = 0; i < 4; ++i) { xv[2 * i] = asf(x0[i] << 16); xv[2 * i + 1] = asf(x0[i] & 0xFFFF0000u); xv[8 + 2 * i] = asf(x1[i] << 16); xv[9 + 2 * i] = asf(x1[i] & 0xFFFF0000u); }
      float s = 0.f;
#pragma unroll
      for (int i = 0; i < 16; ++i) s += xv[i];
      const float mean = sum64(s) * (1.f / D);
      float q2 = 0.f;
#pragma unroll
      for (int i = 0; i < 16; ++i) { const float d = xv[i] - mean; q2 += d * d; }
      const float var = rbf(sum64(q2) * (1.f / D));
      const float sd = rbf(__builtin_sqrtf(rbf(var + 1e-5f)));
      const float nrm = rbf(rbf(asf(xe << 16) - rbf(mean)) / sd);
      asm volatile("s_waitcnt vmcnt(1)" ::: "memory");
      STAMP(1);
      const int off[1] = {WS_H1 + (((lane >> 3) & 3) * 32 + (lane >> 5) * 16 + 4 * kb + (w >> 1)) * 256 + (w & 1) * 128 + c8 * 16};
      v4u u[1]; gather<1>(ws, off, u, tsh);
      STAMP(2);
      const F2 sc = red_pair(unpack4(u[0]));
      const unsigned xx = dup16(rbf(nrm * rbf(1.f + rbf(sc.b))) + rbf(sc.a));
      f32x4 acc = {0, 0, 0, 0};
#define GST(M) acc = mfma(diag<M>(xx, dmask), wv[M], acc);
      GST(0) GST(1) GST(2) GST(3) GST(4) GST(5) GST(6) GST(7)
      if (lane < 32) lp<f32x4>(L, L_RED0)[w * 32 + 2 * c16 + h] = acc;
      bar();
      if (w == 0) final32(L, L_RED0, 32, 0, ws, WS_H2 + ((g * 8 + hd) * 4 + kb) * 512, lane, tag);
      STAMP(3);
    }
    {
      const int pa = pj & 3, wd = pj >> 2;
      const rsrc_t ro = mk(a.wo, 4096 * D * 2);
      v4u wv[8];
      const int vo = ((g * 8 + 2 * pa) * 128 + 16 * wd + h) * 2048 + w * 256 + c16 * 16;
#pragma unroll
      for (int m = 0; m < 8; ++m) wv[m] = ldw(ro, vo, ((m >> 2) * 128 + (m & 3) * 4) * 2048);
      v4u a2w[4]; v2u a2c;
      a2_issue(a2w, a2c);
      SB();
      a2_finish(a2w, a2c, L_RED1);
      STAMP(4);
      const int off[1] = {lane < 32 ? WS_H3D + ((g * 32 + lane) * 32 + wd * 4 + pa) * 128 + w * 16 : WS_H3S + (g * 32 + lane - 32) * 128 + pa * 16};
      v4u u[1]; gather<1>(ws, off, u, tsh);
      STAMP(5);
      const f32x4 v = unpack4(u[0]);
      const float m = sw32(w < 4 ? v[0] : v[2], w < 4 ? v[0] : v[2]).b, l = sw32(w < 4 ? v[1] : v[3], w < 4 ? v[1] : v[3]).b;
      const float M = max32(m);
      const float wg = __builtin_amdgcn_exp2f((m - M) * 1.4426950408889634f);
      const float Lt = sum32(wg * l);
      const float t0 = tr16(wg * v[0], wg * v[2]), t1 = tr16(wg * v[1], wg * v[3]);
      const bool b3 = lane & 8;
      float o = (b3 ? t1 : t0) + dpp<ROR8>(b3 ? t0 : t1);
      o = sum8(o);
      if (lane < 32 && c8 == 0) lp<unsigned>(L, L_O)[4 * w + (lane >> 3)] = dup16(o / Lt);
      bar();
      const unsigned xx = lp<unsigned>(L, L_O)[4 * c8 + h];
      f32x4 acc = {0, 0, 0, 0};
      GST(0) GST(1) GST(2) GST(3) GST(4) GST(5) GST(6) GST(7)
      if (lane < 32) sth(ws, WS_H4A + (P * 32 + pj) * 4096 + (w * 32 + 2 * c16 + h) * 16, pack4(acc, tag));
      STAMP(6);
    }
    if (!isR) {
      const int f = xcd * 14 + j;
      const rsrc_t rd = mk(a.wd, FF * D * 2);
      v4u wv[8];
      const int vo = (32 * f + h) * 2048 + w * 256 + c16 * 16;
#pragma unroll
      for (int m = 0; m < 8; ++m) wv[m] = ldw(rd, vo, m * 8192);
      SB();
      asm volatile("s_waitcnt vmcnt(1)" ::: "memory");
      STAMP(7);
      const int off[1] = {WS_H5 + (((lane >> 3) & 3) * 28 + (f >> 2)) * 1024 + (f & 3) * 256 + (lane >> 5) * 128 + c8 * 16};
      v4u u[1]; gather<1>(ws, off, u, tsh);
      STAMP(8);
      const F2 gu = red_pair(unpack4(u[0]));
      const unsigned xx = dup16(rbf(silu(rbf(gu.a))) * rbf(gu.b));
      f32x4 acc = {0, 0, 0, 0};
      GST(0) GST(1) GST(2) GST(3) GST(4) GST(5) GST(6) GST(7)
#undef GST
      if (lane < 32) sth(ws, WS_H6 + f * 4096 + (w * 32 + 2 * c16 + h) * 16, pack4(acc, tag));
      STAMP(9);
      return;
    }
  }
  {
    const int r = xcd * 4 + (isY ? 2 : 0) + (j - 14);
    const rsrc_t rout = mk(a.out, D * 2);
    const int t16 = lane & 15;
    const unsigned xp = __builtin_amdgcn_raw_buffer_load_b32(rx, (32 * r + 2 * t16) * 2, 0, 0);
    if (w == 0 && lane < 16) __builtin_amdgcn_raw_buffer_store_b32(0u, rout, (32 * r + 2 * t16) * 2, 0, 0);
    if (w < 4) {
      const int off[1] = {WS_HA2 + ((lane >> 3) * 32 + 8 * w + (r >> 2)) * 512 + (r & 3) * 128 + c8 * 16};
      v4u u[1]; gather<1>(ws, off, u, tsh);
      const float s = rbf(red8(unpack4(u[0])));
      if (c16 < 8) lp<float>(L, L_VEC)[w * 32 + 4 * c8 + h] = s;
    }
    bar();
    STAMP(10);
    typedef float f32x2 __attribute__((ext_vector_type(2)));
    const f32x2 ga = lp<f32x2>(L, L_VEC)[t16], gb = lp<f32x2>(L, L_VEC)[48 + t16];
    if (w == 1 && lane < 32) {
      const f32x2 v = lp<f32x2>(L, L_VEC)[(lane < 16 ? 32 : 16) + t16];
      const float add = lane < 16 ? 1.f : 0.f;
      sth2(ws, WS_H4B + r * 512 + 256 + lane * 8, pack2(rbf(v[0] + add), rbf(v[1] + add), tag));
    }
    auto colsum = [&](int base, int np, int red) {
      const int p0 = 16 * w + (lane >> 3), p1 = p0 + 8;
      const int off[2] = {base + (p0 < np ? p0 : 0) * 4096 + r * 128 + c8 * 16, base + (p1 < np ? p1 : 0) * 4096 + r * 128 + c8 * 16};
      v4u u[2]; gather<2>(ws, off, u, tsh);
      f32x4 v0 = unpack4(u[0]), v1 = unpack4(u[1]);
      if (16 * w >= np) v0 = f32x4{0, 0, 0, 0};
      if (16 * w + 8 >= np) v1 = f32x4{0, 0, 0, 0};
      const float s = red8(v0 + v1);
      if (c16 < 8) lp<float>(L, red)[(4 * c8 + h) * 8 + w] = s;
      bar();
      const f32x4 q0 = lp<f32x4>(L, red)[4 * t16], q1 = lp<f32x4>(L, red)[4 * t16 + 1], q2 = lp<f32x4>(L, red)[4 * t16 + 2], q3 = lp<f32x4>(L, red)[4 * t16 + 3];
      const f32x4 e = q0 + q1, o = q2 + q3;
      return F2{(e[0] + e[1]) + (e[2] + e[3]), (o[0] + o[1]) + (o[2] + o[3])};
    };
    const F2 at = colsum(WS_H4A, 128, L_R1);
    STAMP(11);
    const float xa = rbf(asf(xp << 16) + rbf(ga[0] * rbf(at.a))), xb = rbf(asf(xp & 0xFFFF0000u) + rbf(ga[1] * rbf(at.b)));
    if (w == 0) {
      const float mr = sum16(xa + xb) * (1.f / 32.f);
      const float da = xa - mr, db = xb - mr;
      const float m2 = sum16(da * da + db * db);
      v2u gq = pack2(xa, xb, tag);
      if (lane >= 16) gq = v2u{asu(lane == 16 ? mr : m2), tsh};
      if (lane < 18) sth2(ws, WS_H4B + r * 512 + lane * 8, gq);
    }
    STAMP(12);
    const F2 y = colsum(WS_H6, 112, L_R2);
    STAMP(13);
    if (w == 0 && lane < 16) {
      const unsigned o = bfbits(xa + rbf(gb[0] * rbf(y.a))) | (bfbits(xb + rbf(gb[1] * rbf(y.b))) << 16);
      __builtin_amdgcn_raw_buffer_store_b32(o, rout, (32 * r + 2 * t16) * 2, 0, 0);
      if (r == 0 && lane == 0) __builtin_amdgcn_raw_buffer_store_b32(tsh, ws, WS_EPOCH + 4, 0, AUX_SC1);
    }
    STAMP(14);
  }
}
}

extern "C" void kernel_launch(void* const* d_in, const int* in_sizes, int n_in, void* d_out, int out_size, void* d_ws, size_t ws_size, hipStream_t stream) {
  Args a;
  a.x = (const u16*)d_in[0]; a.kc = (const u16*)d_in[1]; a.vc = (const u16*)d_in[2]; a.wq = (const u16*)d_in[3]; a.wo = (const u16*)d_in[4];
  a.wg = (const u16*)d_in[6]; a.wu = (const u16*)d_in[7]; a.wd = (const u16*)d_in[8]; a.c = (const u16*)d_in[9]; a.wm = (const u16*)d_in[10];
  a.out = (u16*)d_out; a.ws = (unsigned char*)d_ws;
  hipLaunchKernelGGL(fused_block, dim3(256), dim3(512), 0, stream, a);
}
```

```cpp
#include <hip/hip_runtime.h>
#include <hip/hip_bf16.h>

#define PROF 0

namespace {
typedef unsigned short u16;
typedef unsigned v4u __attribute__((ext_vector_type(4)));
typedef unsigned v2u __attribute__((ext_vector_type(2)));
typedef float f32x4 __attribute__((ext_vector_type(4)));
typedef __bf16 bf16x8 __attribute__((ext_vector_type(8)));
typedef __amdgpu_buffer_rsrc_t rsrc_t;
#define LAS __attribute__((address_space(3)))
#define GAS __attribute__((address_space(1)))

constexpr int D = 1024, SK = 65536, WIN = 4096, FF = 3584;
constexpr int AUX_NT = 2, AUX_SC1 = 16;
constexpr unsigned SPIN_CAP = 1u << 17;
#define POLL_SLEEP 2

constexpr int WS_EPOCH = 0;
constexpr int WS_H1 = 4096;
constexpr int WS_H2 = WS_H1 + 32768;
constexpr int WS_H3S = WS_H2 + 65536;
constexpr int WS_HA2 = WS_H3S + 16384;
constexpr int WS_H4B = WS_HA2 + 131072;
constexpr int WS_H5 = WS_H4B + 16384;
constexpr int WS_H3D = WS_H5 + 114688;
constexpr int WS_H4A = WS_H3D + 524288;
constexpr int WS_H6 = WS_H4A + 524288;
constexpr int WS_END = WS_H6 + 458752;
constexpr int WS_PROF = 4 << 20;

constexpr int L_V = 0;
constexpr int L_RED0 = 32768;
constexpr int L_RED1 = L_RED0 + 8192;
constexpr int L_RED2 = L_RED1 + 8192;
constexpr int L_Q = L_RED2 + 8192;
constexpr int L_P = L_Q + 8 * 272;
constexpr int L_WMAX = L_P + 8 * 272;
constexpr int L_WSUM = L_WMAX + 256;
constexpr int L_O = L_WSUM + 256;
constexpr int L_VEC = L_O + 128;
constexpr int L_R1 = L_VEC + 512;
constexpr int L_R2 = L_R1 + 1024;
constexpr int LDS_BYTES = 84 * 1024;
static_assert(L_R2 + 1024 <= LDS_BYTES, "lds");

struct Args {
  const u16 *x, *kc, *vc, *wq, *wo, *wg, *wu, *wd, *c, *wm;
  u16* out;
  unsigned char* ws;
};

__device__ __forceinline__ rsrc_t mk(const void* p, unsigned bytes) { return __builtin_amdgcn_make_buffer_rsrc((void*)p, 0, bytes, 0x00020000); }
__device__ __forceinline__ v4u ldw(rsrc_t r, int voff, int soff) { return __builtin_amdgcn_raw_buffer_load_b128(r, voff, soff, AUX_NT); }
__device__ __forceinline__ v4u ldh(rsrc_t r, int voff) { return __builtin_amdgcn_raw_buffer_load_b128(r, voff, 0, AUX_SC1); }
__device__ __forceinline__ void sth(rsrc_t r, int voff, v4u v) { __builtin_amdgcn_raw_buffer_store_b128(v, r, voff, 0, AUX_SC1); }
__device__ __forceinline__ void sth2(rsrc_t r, int voff, v2u v) { __builtin_amdgcn_raw_buffer_store_b64(v, r, voff, 0, AUX_SC1); }
__device__ __forceinline__ float asf(unsigned u) { return __builtin_bit_cast(float, u); }
__device__ __forceinline__ unsigned asu(float f) { return __builtin_bit_cast(unsigned, f); }
typedef float f32x2 __attribute__((ext_vector_type(2)));
typedef __bf16 bf16x2 __attribute__((ext_vector_type(2)));
__device__ __forceinline__ unsigned cvtpk(float lo, float hi) { return __builtin_bit_cast(unsigned, __builtin_convertvector(f32x2{lo, hi}, bf16x2)); }
__device__ __forceinline__ unsigned dup16(float f) { return cvtpk(f, f); }
__device__ __forceinline__ float silu(float x) { return x * __builtin_amdgcn_rcpf(1.f + __builtin_amdgcn_exp2f(x * -1.4426950408889634f)); }
__device__ __forceinline__ float fmx(float a, float b) { return __builtin_fmaxf(a, b); }
#define PIN(x) asm volatile("" : "+v"(x))
template <int C> __device__ __forceinline__ unsigned dppu(unsigned x) { return (unsigned)__builtin_amdgcn_mov_dpp((int)x, C, 0xf, 0xf, true); }
template <int C> __device__ __forceinline__ float dpp(float x) { return asf(dppu<C>(asu(x))); }
constexpr int XOR1 = 0xB1, XOR2 = 0x4E, HMIR = 0x141, ROR8 = 0x128, BCAST = 0x150;
struct F2 { float a, b; };
__device__ __forceinline__ F2 sw32(float a, float b) { auto r = __builtin_amdgcn_permlane32_swap(asu(a), asu(b), false, false); return {asf(r[0]), asf(r[1])}; }
__device__ __forceinline__ F2 sw16(float a, float b) { auto r = __builtin_amdgcn_permlane16_swap(asu(a), asu(b), false, false); return {asf(r[0]), asf(r[1])}; }
__device__ __forceinline__ float tr32(float a, float b) { F2 r = sw32(a, b); return r.a + r.b; }
__device__ __forceinline__ float tr16(float a, float b) { F2 r = sw16(a, b); return r.a + r.b; }
__device__ __forceinline__ float sum8(float v) { v += dpp<XOR1>(v); v += dpp<XOR2>(v); v += dpp<HMIR>(v); return v; }
__device__ __forceinline__ float sum16(float v) { v = sum8(v); v += dpp<ROR8>(v); return v; }
__device__ __forceinline__ float sum32(float v) { v = sum16(v); return tr16(v, v); }
__device__ __forceinline__ float sum64(float v) { v = sum32(v); return tr32(v, v); }
__device__ __forceinline__ float max32(float v) { v = fmx(v, dpp<XOR1>(v)); v = fmx(v, dpp<XOR2>(v)); v = fmx(v, dpp<HMIR>(v)); v = fmx(v, dpp<ROR8>(v)); F2 r = sw16(v, v); return fmx(r.a, r.b); }
__device__ __forceinline__ f32x4 mfma(v4u a, v4u b, f32x4 c) { return __builtin_amdgcn_mfma_f32_16x16x32_bf16(__builtin_bit_cast(bf16x8, a), __builtin_bit_cast(bf16x8, b), c, 0, 0, 0); }
__device__ __forceinline__ void bar() { asm volatile("s_waitcnt lgkmcnt(0)" ::: "memory"); __builtin_amdgcn_s_barrier(); asm volatile("" ::: "memory"); }
#define SB() __builtin_amdgcn_sched_barrier(0)

__device__ __forceinline__ v2u pack2(float a, float b, unsigned tag) {
  return v2u{__builtin_amdgcn_alignbit(asu(a) >> 10, asu(b), 10), __builtin_amdgcn_alignbit(tag, asu(a), 20)};
}
__device__ __forceinline__ v4u pack4(f32x4 v, unsigned tag) { const v2u p = pack2(v[0], v[1], tag), q = pack2(v[2], v[3], tag); return v4u{p.x, p.y, q.x, q.y}; }
__device__ __forceinline__ float upa(unsigned lo, unsigned hi) { return asf(__builtin_amdgcn_alignbit(hi, lo, 12)); }
__device__ __forceinline__ float upb(unsigned lo) { return asf(lo << 10); }
__device__ __forceinline__ f32x4 unpack4(v4u u) { return f32x4{upa(u.x, u.y), upb(u.x), upa(u.z, u.w), upb(u.z)}; }
__device__ __forceinline__ unsigned tagbad(v4u u, unsigned tsh) { return (u.y ^ tsh) | (u.w ^ tsh); }

__device__ __forceinline__ void wait_one(rsrc_t ws, int soff, unsigned tsh) {
  for (unsigned spins = 0; spins < SPIN_CAP; ++spins) {
    asm volatile("" ::: "memory");
    const unsigned t = __builtin_amdgcn_readfirstlane(__builtin_amdgcn_raw_buffer_load_b32(ws, 12, soff, AUX_SC1));
    if ((t ^ tsh) < 4096u) break;
    __builtin_amdgcn_s_sleep(POLL_SLEEP);
  }
}
template <int N, bool TRY> __device__ __forceinline__ void gather(rsrc_t ws, int (&off)[N], v4u (&u)[N], unsigned tsh) {
#pragma unroll
  for (int i = 0; i < N; ++i) PIN(off[i]);
  const int soff = __builtin_amdgcn_readfirstlane(off[0]);
  if (!TRY) wait_one(ws, soff, tsh);
  for (unsigned spins = 0;; ++spins) {
    asm volatile("" ::: "memory");
#pragma unroll
    for (int i = 0; i < N; ++i) u[i] = ldh(ws, off[i]);
    unsigned bad = tagbad(u[0], tsh);
#pragma unroll
    for (int i = 1; i < N; ++i) bad |= tagbad(u[i], tsh);
    if (__all(bad < 4096u)) break;
    if (spins > SPIN_CAP) break;
    if (TRY && spins == 0) wait_one(ws, soff, tsh); else __builtin_amdgcn_s_sleep(1);
  }
  asm volatile("" ::: "memory");
}
template <int M> __device__ __forceinline__ v4u diag(unsigned xx, v4u mask) { const unsigned t = dppu<BCAST + M>(xx); return v4u{t & mask.x, t & mask.y, t & mask.z, t & mask.w}; }
__device__ __forceinline__ F2 red_pair(f32x4 v) {
  const F2 p = sw32(v[0], v[2]), q = sw32(v[1], v[3]);
  float s0 = tr16(p.a, q.a), s1 = tr16(p.b, q.b);
  s0 += dpp<ROR8>(s0); s1 += dpp<ROR8>(s1);
  return {s0, s1};
}
__device__ __forceinline__ float red8(f32x4 v) {
  float s = tr16(tr32(v[0], v[2]), tr32(v[1], v[3]));
  s += dpp<ROR8>(s);
  return s;
}
template <class T> __device__ __forceinline__ LAS T* lp(LAS unsigned char* L, int off) { return (LAS T*)(L + off); }

#define STAMP(i) do {} while (0)
#define FLUSH() do {} while (0)

__device__ __forceinline__ void final32(LAS unsigned char* L, int rd, int pitch, rsrc_t ws, int dst, unsigned tag) {
  const f32x4 s0 = *lp<f32x4>(L, rd), s1 = *lp<f32x4>(L, rd + pitch), s2 = *lp<f32x4>(L, rd + 2 * pitch), s3 = *lp<f32x4>(L, rd + 3 * pitch);
  const f32x4 s = (s0 + s1) + (s2 + s3);
  sth2(ws, dst, pack2(tr32(s[0], s[2]), tr32(s[1], s[3]), tag));
}

__global__ void __launch_bounds__(512, 2) fused_block(Args a) {
#pragma clang fp reassociate(off) contract(fast)
  __shared__ __attribute__((aligned(16))) unsigned char smem[LDS_BYTES];
  LAS unsigned char* L = (LAS unsigned char*)smem;
  const int b = blockIdx.x, tid = threadIdx.x, lane = tid & 63, w = __builtin_amdgcn_readfirstlane(tid >> 6);
  const int xcd = b & 7, slot = b >> 3, j = slot >> 1;
  const bool isY = slot & 1;
  const int c16 = lane & 15, h = lane >> 4, c8 = lane & 7, u5 = lane & 31, hf = lane >> 5;
  STAMP(0);
  const rsrc_t ws = mk(a.ws, WS_END), rwm = mk(a.wm, D * 6144 * 2), rc = mk(a.c, D * 2), rx = mk(a.x, D * 2);
  const int P = xcd >> 1, pj = (xcd & 1) * 16 + j;
  const int cg32 = xcd + 8 * (j & 3), ks4 = j >> 2;
  const bool isR = j >= 14;
  constexpr float LOG2E = 1.4426950408889634f;
  v4u pf = {0, 0, 0, 0};
  if (false) {
    unsigned long long pc; asm volatile("s_getpc_b64 %0" : "=s"(pc));
    pf = *(const GAS v4u*)((pc & ~255ull) + (slot & 15) * 1024 + lane * 16);
  }
  const int a2r0 = (isY ? 0 : 512) + 128 * ks4 + 16 * w + 4 * h;
  v4u dmask, dmask2, A2[4];
  unsigned tag, tsh;
  auto setup = [&](v2u a2c) {
    const unsigned ep = *(const __attribute__((address_space(4))) unsigned*)(a.ws + WS_EPOCH + 4);
    tag = (ep >> 12) + 1u & 0xFFFFFu; tsh = tag << 12;
    const unsigned mv = (c16 & 1) ? 0xFFFF0000u : 0x0000FFFFu;
    const int t = c8 >> 1;
    dmask2 = v4u{t == 0 ? mv : 0u, t == 1 ? mv : 0u, t == 2 ? mv : 0u, t == 3 ? mv : 0u};
    dmask = c16 < 8 ? dmask2 : v4u{0, 0, 0, 0};
  };
  auto sdiag = [&](v2u cv, v4u mask, v4u (&A)[4]) {
    const float s0 = silu(asf(cv.x << 16)), s1 = silu(asf(cv.x & 0xFFFF0000u)), s2 = silu(asf(cv.y << 16)), s3 = silu(asf(cv.y & 0xFFFF0000u));
    const unsigned d[4] = {dup16(s0), dup16(s1), dup16(s2), dup16(s3)};
#pragma unroll
    for (int m = 0; m < 4; ++m) A[m] = v4u{d[m] & mask.x, d[m] & mask.y, d[m] & mask.z, d[m] & mask.w};
  };
  auto a2_issue = [&](v4u (&wv)[4]) {
    const int vo = a2r0 * 12288 + 4096 + cg32 * 256 + c16 * 16;
#pragma unroll
    for (int m = 0; m < 4; ++m) wv[m] = ldw(rwm, vo, m * 12288);
  };
  auto a2_finish = [&](v4u (&wv)[4]) {
    f32x4 acc = {0, 0, 0, 0};
#pragma unroll
    for (int m = 0; m < 4; ++m) acc = mfma(A2[m], wv[m], acc);
    if (lane < 32) *lp<f32x4>(L, L_RED1 + w * 512 + (2 * c16 + h) * 16) = acc;
    bar();
    if (w == 0) final32(L, L_RED1 + hf * 2048 + u5 * 16, 512, ws, WS_HA2 + (((isY ? 0 : 4) + ks4) * 32 + cg32) * 512 + u5 * 16 + hf * 8, tag);
  };

  if (!isY) {
    {
      const int r0 = 256 * ks4 + 32 * w + 4 * (2 * h + (c16 >> 3));
      const v2u cv = __builtin_amdgcn_raw_buffer_load_b64(rc, r0 * 2, 0, 0);
      const v2u a2c = __builtin_amdgcn_raw_buffer_load_b64(rc, a2r0 * 2, 0, 0);
      v4u wv[4];
      const int vo = r0 * 12288 + cg32 * 128 + c8 * 16;
#pragma unroll
      for (int m = 0; m < 4; ++m) wv[m] = ldw(rwm, vo, m * 12288);
      SB();
      setup(a2c);
      v4u A[4]; sdiag(cv, dmask2, A);
      int wr = L_RED0 + (w * 2 + (h >> 1)) * 256 + (2 * c8 + (h & 1)) * 16, rd = L_RED0 + h * 1024 + c16 * 16;
      int dst = WS_H1 + (ks4 * 32 + cg32) * 256 + c16 * 16 + (h >> 1) * 8;
      PIN(wr); PIN(rd); PIN(dst);
      asm volatile("" :: "v"(pf));
      SB();
      f32x4 acc = {0, 0, 0, 0};
#pragma unroll
      for (int m = 0; m < 4; ++m) acc = mfma(A[m], wv[m], acc);
      if ((c16 >> 3) == (h >> 1)) *lp<f32x4>(L, wr) = acc;
      STAMP(1);
      bar();
      if (w == 0) {
        const f32x4 s0 = *lp<f32x4>(L, rd), s1 = *lp<f32x4>(L, rd + 256), s2 = *lp<f32x4>(L, rd + 512), s3 = *lp<f32x4>(L, rd + 768);
        const f32x4 s = (s0 + s1) + (s2 + s3);
        float e0 = tr32(s[0], s[2]), e1 = tr32(s[1], s[3]);
        e0 = tr16(e0, e0); e1 = tr16(e1, e1);
        if (!(h & 1)) sth2(ws, dst, pack2(e0, e1, tag));
      }
      STAMP(2);
      SB();
      const int g = P, chunk = pj;
      v4u kf[4], vf[4];
      const rsrc_t rk = mk(a.kc + (size_t)(g * SK + SK - WIN + chunk * 128) * 128, 32768), rv = mk(a.vc + (size_t)(g * SK + SK - WIN + chunk * 128) * 128, 32768);
      v4u vv[4];
#pragma unroll
      for (int t = 0; t < 4; ++t) vv[t] = ldw(rv, w * 4096 + lane * 16, t * 1024);
#pragma unroll
      for (int s = 0; s < 4; ++s) kf[s] = ldw(rk, (16 * w + c16) * 256 + h * 16, s * 64);
      SB();
      sdiag(a2c, dmask, A2);
#pragma unroll
      for (int t = 0; t < 4; ++t) { const int row = 16 * w + 4 * t + h; *lp<v4u>(L, L_V + row * 256 + 16 * (c16 ^ ((h << 2) | t))) = vv[t]; }
      bar();
#pragma unroll
      for (int s = 0; s < 4; ++s) {
        v2u lo, hi;
        const int q = c16 >> 2, p = c16 & 3, vr = 32 * s + 8 * h + q, ch = 2 * w + (p >> 1);
        const int a0 = L_V + vr * 256 + 16 * (ch ^ ((q << 2) | ((2 * h) & 3))) + 8 * (p & 1);
        const int a1 = L_V + (vr + 4) * 256 + 16 * (ch ^ ((q << 2) | ((2 * h + 1) & 3))) + 8 * (p & 1);
        const unsigned base = (unsigned)(size_t)L;
        asm volatile("ds_read_b64_tr_b16 %0, %2\n\tds_read_b64_tr_b16 %1, %3\n\ts_waitcnt lgkmcnt(0)" : "=&v"(lo), "=&v"(hi) : "v"(base + a0), "v"(base + a1) : "memory");
        vf[s] = v4u{lo.x, lo.y, hi.x, hi.y};
      }
      int qw = L_Q + w * 272 + u5 * 8 + hf * 4, fr = c8 * 272 + h * 16, mw = L_WMAX + c8 * 32 + w * 4, pw = L_P + c8 * 272 + w * 32 + h * 8;
      int od = WS_H3D + ((g * 32 + chunk) * 32 + w * 4 + (c8 >> 1)) * 128 + ((c16 & 1) * 4 + h) * 16, sd = WS_H3S + (g * 32 + chunk) * 128 + c8 * 8;
      PIN(qw); PIN(fr); PIN(mw); PIN(pw); PIN(od); PIN(sd);
      asm volatile("s_waitcnt vmcnt(0)" ::: "memory");
      STAMP(3);
      int off[2] = {WS_H2 + ((g * 8 + w) * 4 + hf) * 512 + u5 * 16, WS_H2 + ((g * 8 + w) * 4 + 2 + hf) * 512 + u5 * 16};
      v4u u[2]; gather<2, false>(ws, off, u, tsh);
      STAMP(4);
      const f32x4 qs = unpack4(u[0]) + unpack4(u[1]);
      *lp<unsigned>(L, qw) = cvtpk(tr32(qs[0], qs[2]), tr32(qs[1], qs[3]));
      bar();
      const v4u q0 = *lp<v4u>(L, L_Q + fr), q1 = *lp<v4u>(L, L_Q + fr + 64), q2 = *lp<v4u>(L, L_Q + fr + 128), q3 = *lp<v4u>(L, L_Q + fr + 192);
      SB();
      f32x4 sc = mfma(kf[0], q0, f32x4{0, 0, 0, 0});
      sc = mfma(kf[1], q1, sc); sc = mfma(kf[2], q2, sc); sc = mfma(kf[3], q3, sc);
      float mx = fmx(fmx(sc[0], sc[1]), fmx(sc[2], sc[3]));
      { F2 t = sw16(mx, mx); mx = fmx(t.a, t.b); t = sw32(mx, mx); mx = fmx(t.a, t.b); }
      if (lane < 8) *lp<float>(L, mw) = mx;
      bar();
      const f32x4 m0 = *lp<f32x4>(L, L_WMAX + c8 * 32), m1 = *lp<f32x4>(L, L_WMAX + c8 * 32 + 16);
      constexpr float C2 = 0.08838834764831845f * LOG2E;
      const float m = asf(asu(C2 * fmx(fmx(fmx(m0[0], m0[1]), fmx(m0[2], m0[3])), fmx(fmx(m1[0], m1[1]), fmx(m1[2], m1[3])))) & 0xFFFFFC00u);
      const float p0 = __builtin_amdgcn_exp2f(sc[0] * C2 - m), p1 = __builtin_amdgcn_exp2f(sc[1] * C2 - m), p2 = __builtin_amdgcn_exp2f(sc[2] * C2 - m), p3 = __builtin_amdgcn_exp2f(sc[3] * C2 - m);
      if (c16 < 8) *lp<v2u>(L, pw) = v2u{cvtpk(p0, p1), cvtpk(p2, p3)};
      bar();
      const v4u b0 = *lp<v4u>(L, L_P + fr), b1 = *lp<v4u>(L, L_P + fr + 64), b2 = *lp<v4u>(L, L_P + fr + 128), b3 = *lp<v4u>(L, L_P + fr + 192);
      SB();
      f32x4 o = mfma(vf[0], b0, f32x4{0, 0, 0, 0});
      o = mfma(vf[1], b1, o); o = mfma(vf[2], b2, o); o = mfma(vf[3], b3, o);
      if (c16 < 8) sth(ws, od, pack4(o, tag));
      if (w == 7) {
        const v4u one = {0x3F803F80u, 0x3F803F80u, 0x3F803F80u, 0x3F803F80u};
        f32x4 l = mfma(one, b0, f32x4{0, 0, 0, 0});
        l = mfma(one, b1, l); l = mfma(one, b2, l); l = mfma(one, b3, l);
        if (lane < 8) sth2(ws, sd, pack2(l[0], m, tag));
      }
      STAMP(5);
      SB();
    }
    v4u a2w[4];
    a2_issue(a2w);
    if (!isR) {
      const int e = xcd * 14 + j, cg = e % 28, ks = e / 28;
      const rsrc_t rg = mk(a.wg, D * FF * 2), ru = mk(a.wu, D * FF * 2);
      v4u gw[8], uw[8];
      const int vo = (256 * ks + 32 * w + h) * (FF * 2) + cg * 256 + c16 * 16;
#pragma unroll
      for (int m = 0; m < 8; ++m) { gw[m] = ldw(rg, vo, m * 4 * FF * 2); uw[m] = ldw(ru, vo, m * 4 * FF * 2); }
      SB();
      a2_finish(a2w);
      STAMP(6);
      int wr = L_RED2 + w * 1024 + (2 * c16 + h) * 16, rd = L_RED2 + hf * 4096 + (w & 1) * 512 + u5 * 16;
      int dst = WS_H5 + (ks * 28 + cg) * 1024 + (u5 >> 3) * 256 + (w & 1) * 128 + c8 * 16 + hf * 8;
      PIN(wr); PIN(rd); PIN(dst);
      const int rb = WS_H4B + (8 * ks + w) * 512 + c8 * 16 + (h >> 1) * 8;
      float c1, sb;
      {
        int off[2] = {rb + 256 - (h >> 1) * 8, rb + 384 - (h >> 1) * 8};
        v4u u[2]; gather<2, true>(ws, off, u, tsh);
        const unsigned l0 = (h & 2) ? u[0].z : u[0].x, h0 = (h & 2) ? u[0].w : u[0].y, l1 = (h & 2) ? u[1].z : u[1].x, h1 = (h & 2) ? u[1].w : u[1].y;
        c1 = (h & 1) ? upb(l0) : upa(l0, h0); sb = (h & 1) ? upb(l1) : upa(l1, h1);
      }
      STAMP(7);
      int off[2] = {rb - (h >> 1) * 8, WS_H4B + u5 * 512 + 128};
      v4u u[2]; gather<2, true>(ws, off, u, tsh);
      STAMP(8);
      const float mean = sum32(asf(u[1].x)) * (1.f / D), ex2 = sum32(asf(u[1].z)) * (1.f / D);
      const unsigned l0 = (h & 2) ? u[0].z : u[0].x, h0 = (h & 2) ? u[0].w : u[0].y;
      const float x1 = (h & 1) ? upb(l0) : upa(l0, h0);
      const float rstd = __builtin_amdgcn_rsqf(fmx(ex2 - mean * mean, 0.f) + 1e-5f);
      const unsigned xx = dup16((x1 - mean) * c1 * rstd + sb);
      f32x4 ag = {0, 0, 0, 0}, au = {0, 0, 0, 0};
#define EST(M) { const v4u A = diag<M>(xx, dmask); ag = mfma(A, gw[M], ag); au = mfma(A, uw[M], au); }
      EST(0) EST(1) EST(2) EST(3) EST(4) EST(5) EST(6) EST(7)
#undef EST
      if (lane < 32) { *lp<f32x4>(L, wr) = ag; *lp<f32x4>(L, wr + 512) = au; }
      bar();
      if (w < 2) final32(L, rd, 1024, ws, dst, tag);
      STAMP(9);
      FLUSH();
      return;
    }
    SB();
    a2_finish(a2w);
    STAMP(6);
  } else {
    const int g = (P + 3) & 3;
    {
      const int hd = pj >> 2, kb = pj & 3;
      const rsrc_t rq = mk(a.wq, D * 4096 * 2);
      const v4u x0 = __builtin_amdgcn_raw_buffer_load_b128(rx, lane * 32, 0, 0), x1 = __builtin_amdgcn_raw_buffer_load_b128(rx, lane * 32 + 16, 0, 0);
      const unsigned xe = __builtin_amdgcn_raw_buffer_load_b16(rx, (256 * kb + 32 * w + 4 * c8 + h) * 2, 0, 0);
      const v2u a2c = __builtin_amdgcn_raw_buffer_load_b64(rc, a2r0 * 2, 0, 0);
      v4u wv[8];
      const int vo = (256 * kb + 32 * w + h) * 8192 + (g * 8 + hd) * 256 + c16 * 16;
#pragma unroll
      for (int m = 0; m < 8; ++m) wv[m] = ldw(rq, vo, m * 32768);
      SB();
      setup(a2c);
      asm volatile("" :: "v"(pf));
      float xv[16];
#pragma unroll
      for (int i = 0; i < 4; ++i) { xv[2 * i] = asf(x0[i] << 16); xv[2 * i + 1] = asf(x0[i] & 0xFFFF0000u); xv[8 + 2 * i] = asf(x1[i] << 16); xv[9 + 2 * i] = asf(x1[i] & 0xFFFF0000u); }
      float s = 0.f;
#pragma unroll
      for (int i = 0; i < 16; ++i) s += xv[i];
      const float mean = sum64(s) * (1.f / D);
      float q2 = 0.f;
#pragma unroll
      for (int i = 0; i < 16; ++i) { const float d = xv[i] - mean; q2 += d * d; }
      float nrm = (asf(xe << 16) - mean) * __builtin_amdgcn_rsqf(sum64(q2) * (1.f / D) + 1e-5f);
      sdiag(a2c, dmask, A2);
      int wr = L_RED0 + w * 512 + (2 * c16 + h) * 16, rd = L_RED0 + hf * 2048 + u5 * 16, dst = WS_H2 + ((g * 8 + hd) * 4 + kb) * 512 + u5 * 16 + hf * 8;
      PIN(wr); PIN(rd); PIN(dst); PIN(nrm);
      asm volatile("s_waitcnt vmcnt(1)" ::: "memory");
      STAMP(1);
      int off[1] = {WS_H1 + (((lane >> 3) & 3) * 32 + hf * 16 + 4 * kb + (w >> 1)) * 256 + (w & 1) * 128 + c8 * 16};
      v4u u[1]; gather<1, true>(ws, off, u, tsh);
      STAMP(2);
      const F2 sc = red_pair(unpack4(u[0]));
      const unsigned xx = dup16(nrm * sc.b + (nrm + sc.a));
      f32x4 acc = {0, 0, 0, 0};
#define GST(M) acc = mfma(diag<M>(xx, dmask), wv[M], acc);
      GST(0) GST(1) GST(2) GST(3) GST(4) GST(5) GST(6) GST(7)
      if (lane < 32) *lp<f32x4>(L, wr) = acc;
      bar();
      if (w == 0) final32(L, rd, 512, ws, dst, tag);
      STAMP(3);
      SB();
    }
    {
      const int pa = pj & 3, wd = pj >> 2;
      const rsrc_t ro = mk(a.wo, 4096 * D * 2);
      v4u wv[8];
      const int vo = ((g * 8 + 2 * pa) * 128 + 16 * wd + h) * 2048 + w * 256 + c16 * 16;
#pragma unroll
      for (int m = 0; m < 8; ++m) wv[m] = ldw(ro, vo, ((m >> 2) * 128 + (m & 3) * 4) * 2048);
      v4u a2w[4];
      a2_issue(a2w);
      SB();
      a2_finish(a2w);
      int ow = L_O + (4 * w + (lane >> 3)) * 4, orr = L_O + (4 * c8 + h) * 4, dst = WS_H4A + (P * 32 + pj) * 4096 + (w * 32 + 2 * c16 + h) * 16;
      PIN(ow); PIN(orr); PIN(dst);
      STAMP(4);
      int off[1] = {lane < 32 ? WS_H3D + ((g * 32 + lane) * 32 + wd * 4 + pa) * 128 + w * 16 : WS_H3S + (g * 32 + lane - 32) * 128 + pa * 16};
      v4u u[1]; gather<1, false>(ws, off, u, tsh);
      STAMP(5);
      const f32x4 v = unpack4(u[0]);
      const float ls = w < 4 ? v[0] : v[2], ms = w < 4 ? v[1] : v[3];
      const float m = sw32(ms, ms).b, l = sw32(ls, ls).b;
      const float wg = __builtin_amdgcn_exp2f(m - max32(m));
      const float Lt = sum32(wg * l);
      const float t0 = tr16(wg * v[0], wg * v[2]), t1 = tr16(wg * v[1], wg * v[3]);
      const bool b3 = lane & 8;
      float o = (b3 ? t1 : t0) + dpp<ROR8>(b3 ? t0 : t1);
      o = sum8(o);
      if (lane < 32 && c8 == 0) *lp<unsigned>(L, ow) = dup16(o * __builtin_amdgcn_rcpf(Lt));
      bar();
      const unsigned xx = *lp<unsigned>(L, orr);
      f32x4 acc = {0, 0, 0, 0};
      GST(0) GST(1) GST(2) GST(3) GST(4) GST(5) GST(6) GST(7)
      if (lane < 32) sth(ws, dst, pack4(acc, tag));
      STAMP(6);
      SB();
    }
    if (!isR) {
      const int f = xcd * 14 + j;
      const rsrc_t rd = mk(a.wd, FF * D * 2);
      v4u wv[8];
      const int vo = (32 * f + h) * 2048 + w * 256 + c16 * 16;
#pragma unroll
      for (int m = 0; m < 8; ++m) wv[m] = ldw(rd, vo, m * 8192);
      SB();
      int dst = WS_H6 + f * 4096 + (w * 32 + 2 * c16 + h) * 16;
      PIN(dst);
      asm volatile("s_waitcnt vmcnt(1)" ::: "memory");
      STAMP(7);
      int off[1] = {WS_H5 + (((lane >> 3) & 3) * 28 + (f >> 2)) * 1024 + (f & 3) * 256 + hf * 128 + c8 * 16};
      v4u u[1]; gather<1, true>(ws, off, u, tsh);
      STAMP(8);
      const F2 gu = red_pair(unpack4(u[0]));
      const unsigned xx = dup16(silu(gu.a) * gu.b);
      f32x4 acc = {0, 0, 0, 0};
      GST(0) GST(1) GST(2) GST(3) GST(4) GST(5) GST(6) GST(7)
#undef GST
      if (lane < 32) sth(ws, dst, pack4(acc, tag));
      STAMP(9);
      FLUSH();
      return;
    }
  }
  {
    const int r = xcd * 4 + (isY ? 2 : 0) + (j - 14);
    const rsrc_t rout = mk(a.out, D * 2);
    const unsigned xp = __builtin_amdgcn_raw_buffer_load_b32(rx, (32 * r + 2 * c16) * 2, 0, 0);
    if (w == 0 && lane < 16) __builtin_amdgcn_raw_buffer_store_b32(0u, rout, (32 * r + 2 * c16) * 2, 0, 0);
    if (w < 4) {
      int off[1] = {WS_HA2 + ((lane >> 3) * 32 + 8 * w + (r >> 2)) * 512 + (r & 3) * 128 + c8 * 16};
      v4u u[1]; gather<1, false>(ws, off, u, tsh);
      const float s = red8(unpack4(u[0]));
      if (c16 < 8) lp<float>(L, L_VEC)[w * 32 + 4 * c8 + h] = s;
    }
    bar();
    STAMP(10);
    const f32x2 ga = lp<f32x2>(L, L_VEC)[c16], gb = lp<f32x2>(L, L_VEC)[48 + c16];
    if (w == 1 && lane < 32) {
      const f32x2 v = lp<f32x2>(L, L_VEC)[(lane < 16 ? 32 : 16) + c16];
      const float add = lane < 16 ? 1.f : 0.f;
      sth2(ws, WS_H4B + r * 512 + 256 + lane * 8, pack2(v[0] + add, v[1] + add, tag));
    }
    auto colsum = [&](int base, int np, int red) {
      const int p0 = 16 * w + (lane >> 3), p1 = p0 + 8;
      int wr = red + (4 * c8 + h) * 32 + w * 4, rd = red + c16 * 64;
      PIN(wr); PIN(rd);
      if (16 * w < np) {
        int off[2] = {base + p0 * 4096 + r * 128 + c8 * 16, base + p1 * 4096 + r * 128 + c8 * 16};
        v4u u[2]; gather<2, false>(ws, off, u, tsh);
        const float s = red8(unpack4(u[0]) + unpack4(u[1]));
        if (c16 < 8) *lp<float>(L, wr) = s;
      } else if (c16 < 8) *lp<float>(L, wr) = 0.f;
      bar();
      const f32x4 q0 = *lp<f32x4>(L, rd), q1 = *lp<f32x4>(L, rd + 16), q2 = *lp<f32x4>(L, rd + 32), q3 = *lp<f32x4>(L, rd + 48);
      const f32x4 e = q0 + q1, o = q2 + q3;
      return F2{(e[0] + e[1]) + (e[2] + e[3]), (o[0] + o[1]) + (o[2] + o[3])};
    };
    int dst = WS_H4B + r * 512 + lane * 8;
    PIN(dst);
    const F2 at = colsum(WS_H4A, 128, L_R1);
    STAMP(11);
    const float xa = asf(xp << 16) + ga[0] * at.a, xb = asf(xp & 0xFFFF0000u) + ga[1] * at.b;
    if (w == 0) {
      const float s1 = sum16(xa + xb), s2 = sum16(xa * xa + xb * xb);
      v2u gq = pack2(xa, xb, tag);
      if (lane >= 16) gq = v2u{asu(lane == 16 ? s1 : s2), tsh};
      if (lane < 18) sth2(ws, dst, gq);
    }
    STAMP(12);
    const F2 y = colsum(WS_H6, 112, L_R2);
    STAMP(13);
    if (w == 0 && lane < 16) {
      __builtin_amdgcn_raw_buffer_store_b32(cvtpk(xa + gb[0] * y.a, xb + gb[1] * y.b), rout, (32 * r + 2 * c16) * 2, 0, 0);
      if (r == 0 && lane == 0) __builtin_amdgcn_raw_buffer_store_b32(tsh, ws, WS_EPOCH + 4, 0, AUX_SC1);
    }
    STAMP(14);
    FLUSH();
  }
}
}

extern "C" void kernel_launch(void* const* d_in, const int* in_sizes, int n_in, void* d_out, int out_size, void* d_ws, size_t ws_size, hipStream_t stream) {
  Args a;
  a.x = (const u16*)d_in[0]; a.kc = (const u16*)d_in[1]; a.vc = (const u16*)d_in[2]; a.wq = (const u16*)d_in[3]; a.wo = (const u16*)d_in[4];
  a.wg = (const u16*)d_in[6]; a.wu = (const u16*)d_in[7]; a.wd = (const u16*)d_in[8]; a.c = (const u16*)d_in[9]; a.wm = (const u16*)d_in[10];
  a.out = (u16*)d_out; a.ws = (unsigned char*)d_ws;
  hipLaunchKernelGGL(fused_block, dim3(256), dim3(512), 0, stream, a);
}
```

```cpp
#include <hip/hip_runtime.h>
#include <hip/hip_bf16.h>

#define PROF 0
#define KNOB(n, v)
#define X_DELAY 22
#define H3WAIT 21
#define H4WAIT 0
#define F_DELAY 30
#define BEARLY 4
#define CODE_BYTES 14600
#define CX 2
#define RSLEEP 0
#define L5 true
#define L4B false
#define LR false
#define L2 false
#define L1 false
#define OUT0 0
#define PRIO 0
#define U5 0
#define U6 0
#define U4B 0
#define U4A 0
#define SPEC 0
#define ALLSTAT 0

namespace {
typedef unsigned short u16;
typedef unsigned v4u __attribute__((ext_vector_type(4)));
typedef unsigned v2u __attribute__((ext_vector_type(2)));
typedef float f32x4 __attribute__((ext_vector_type(4)));
typedef float f32x2 __attribute__((ext_vector_type(2)));
typedef __bf16 bf16x8 __attribute__((ext_vector_type(8)));
typedef __bf16 bf16x2 __attribute__((ext_vector_type(2)));
typedef __amdgpu_buffer_rsrc_t rsrc_t;
#define LAS __attribute__((address_space(3)))
#define GAS __attribute__((address_space(1)))
#define CAS __attribute__((address_space(4)))

constexpr int D = 1024, SK = 65536, WIN = 4096, FF = 3584;
constexpr int AUX_NT = 19, AUX_SC1 = 16;
constexpr unsigned SPIN_CAP = 1u << 17;
constexpr float LOG2E = 1.4426950408889634f;

constexpr int WS_EPOCH = 0;
constexpr int WS_H1 = 4096;
constexpr int WS_H2 = WS_H1 + 65536;
constexpr int WS_H3S = WS_H2 + 65536;
constexpr int WS_HGA = WS_H3S + 16384;
constexpr int WS_HGB = WS_HGA + 65536;
constexpr int WS_HSC = WS_HGB + 65536;
constexpr int WS_H4B = WS_HSC + 65536;
constexpr int WS_H5 = WS_H4B + 8192;
constexpr int WS_H3D = WS_H5 + 229376;
constexpr int WS_H4A = WS_H3D + 524288;
constexpr int WS_H6 = WS_H4A + 131072;
constexpr int WS_END = WS_H6 + 114688;
constexpr int WS_PROF = 4 << 20;

constexpr int L_RED0 = 0;
constexpr int L_RED1 = L_RED0 + 4096;
constexpr int L_RED2 = L_RED1 + 4096;
constexpr int L_Q = L_RED2 + 8192;
constexpr int L_P = L_Q + 8 * 272;
constexpr int L_WMAX = L_P + 8 * 272;
constexpr int L_ST = L_WMAX + 32;
constexpr int L_TAG = L_ST + 8;
constexpr int L_V = 24576;
constexpr int L_K = L_V + 32768;
constexpr int LDS_BYTES = L_K + 32768;
static_assert(L_ST + 16 <= L_V && LDS_BYTES > 81920, "lds");

struct Args {
  const u16 *c, *wm, *wq;
  unsigned char* ws;
  const u16 *x, *kc, *vc, *wo, *wg, *wu, *wd;
  u16* out;
};

__device__ __forceinline__ rsrc_t mk(const void* p, unsigned bytes) { return __builtin_amdgcn_make_buffer_rsrc((void*)p, 0, bytes, 0x00020000); }
__device__ __forceinline__ v4u ldw(rsrc_t r, int voff, int soff) { return __builtin_amdgcn_raw_buffer_load_b128(r, voff, soff, AUX_NT); }
__device__ __forceinline__ v4u ldh(rsrc_t r, int voff) { return __builtin_amdgcn_raw_buffer_load_b128(r, voff, 0, AUX_SC1); }
__device__ __forceinline__ void sth(rsrc_t r, int voff, v4u v) { __builtin_amdgcn_raw_buffer_store_b128(v, r, voff, 0, AUX_SC1); }
__device__ __forceinline__ void sth2(rsrc_t r, int voff, v2u v) { __builtin_amdgcn_raw_buffer_store_b64(v, r, voff, 0, AUX_SC1); }
__device__ __forceinline__ float asf(unsigned u) { return __builtin_bit_cast(float, u); }
__device__ __forceinline__ unsigned asu(float f) { return __builtin_bit_cast(unsigned, f); }
__device__ __forceinline__ unsigned cvtpk(float lo, float hi) { return __builtin_bit_cast(unsigned, __builtin_convertvector(f32x2{lo, hi}, bf16x2)); }
__device__ __forceinline__ unsigned dup16(float f) { return cvtpk(f, f); }
__device__ __forceinline__ float blo(unsigned p) { return asf(p << 16); }
__device__ __forceinline__ float bhi(unsigned p) { return asf(p & 0xFFFF0000u); }
__device__ __forceinline__ float rbf(float f) { return bhi(cvtpk(f, f)); }
__device__ __forceinline__ float silu(float x) { return x * __builtin_amdgcn_rcpf(1.f + __builtin_amdgcn_exp2f(x * -LOG2E)); }
__device__ __forceinline__ float max3(float a, float b, float c) { float r; asm("v_max3_f32 %0, %1, %2, %3" : "=v"(r) : "v"(a), "v"(b), "v"(c)); return r; }
__device__ __forceinline__ float add1(float a, float b) { float r; asm("v_add_f32 %0, %1, %2" : "=v"(r) : "v"(a), "v"(b)); return r; }
#define PIN(x) asm volatile("" : "+v"(x))
#define PINS(x) asm volatile("" : "+s"(x))
template <int C> __device__ __forceinline__ unsigned dppu(unsigned x) { return (unsigned)__builtin_amdgcn_mov_dpp((int)x, C, 0xf, 0xf, true); }
template <int C> __device__ __forceinline__ float dpp(float x) { return asf(dppu<C>(asu(x))); }
constexpr int ROR4 = 0x124, QB0 = 0x00, QB1 = 0x55, XOR1 = 0xB1, XOR2 = 0x4E, HMIR = 0x141, ROR8 = 0x128, BCAST = 0x150;
struct F2 { float a, b; };
__device__ __forceinline__ F2 sw32(float a, float b) { auto r = __builtin_amdgcn_permlane32_swap(asu(a), asu(b), false, false); return {asf(r[0]), asf(r[1])}; }
__device__ __forceinline__ F2 sw16(float a, float b) { auto r = __builtin_amdgcn_permlane16_swap(asu(a), asu(b), false, false); return {asf(r[0]), asf(r[1])}; }
__device__ __forceinline__ float tr32(float a, float b) { F2 r = sw32(a, b); return r.a + r.b; }
__device__ __forceinline__ float tr16(float a, float b) { F2 r = sw16(a, b); return r.a + r.b; }
__device__ __forceinline__ float sum8(float v) { v += dpp<XOR1>(v); v += dpp<XOR2>(v); v += dpp<HMIR>(v); return v; }
__device__ __forceinline__ float sum16(float v) { v = sum8(v); v += dpp<ROR8>(v); return v; }
__device__ __forceinline__ float sum32(float v) { v = sum16(v); return tr16(v, v); }
__device__ __forceinline__ float sum64(float v) { v = sum32(v); return tr32(v, v); }
__device__ __forceinline__ float maxb(float v) {
  float t;
  asm("s_nop 1\n\tv_max_f32_dpp %0, %0, %0 quad_perm:[1,0,3,2] row_mask:0xf bank_mask:0xf\n\t"
      "s_nop 1\n\tv_max_f32_dpp %0, %0, %0 row_ror:8 row_mask:0xf bank_mask:0xf\n\t"
      "s_nop 1\n\tv_max_f32_dpp %0, %0, %0 row_ror:4 row_mask:0xf bank_mask:0xf\n\t"
      "v_mov_b32 %1, %0\n\ts_nop 1\n\tv_permlane16_swap_b32 %0, %1\n\tv_max_f32 %0, %0, %1\n\t"
      "v_mov_b32 %1, %0\n\ts_nop 1\n\tv_permlane32_swap_b32 %0, %1\n\tv_max_f32 %0, %0, %1"
      : "+v"(v), "=&v"(t));
  return v;
}
__device__ __forceinline__ f32x4 mfma(v4u a, v4u b, f32x4 c) { return __builtin_amdgcn_mfma_f32_16x16x32_bf16(__builtin_bit_cast(bf16x8, a), __builtin_bit_cast(bf16x8, b), c, 0, 0, 0); }
__device__ __forceinline__ void bar() { asm volatile("s_waitcnt lgkmcnt(0)" ::: "memory"); __builtin_amdgcn_s_barrier(); asm volatile("" ::: "memory"); }
#define SB() __builtin_amdgcn_sched_barrier(0)
#define DRAIN() asm volatile("s_waitcnt vmcnt(0)" ::: "memory")
#define IBAR() do { SB(); __builtin_amdgcn_s_barrier(); SB(); } while (0)

template <bool RN> __device__ __forceinline__ v2u pack2(float a, float b, unsigned tag) {
  const unsigned ua = asu(a) + (RN ? 0x200u : 0u), ub = asu(b) + (RN ? 0x200u : 0u);
  return v2u{__builtin_amdgcn_alignbit(ua >> 10, ub, 10), __builtin_amdgcn_alignbit(tag, ua, 20)};
}
__device__ __forceinline__ v4u pack4(f32x4 v, unsigned tag) { const v2u p = pack2<false>(v[0], v[1], tag), q = pack2<false>(v[2], v[3], tag); return v4u{p.x, p.y, q.x, q.y}; }
template <bool EX> __device__ __forceinline__ float upa(unsigned lo, unsigned hi) { return asf(__builtin_amdgcn_alignbit(hi, lo, 12) & (EX ? 0xFFFFFC00u : 0xFFFFFFFFu)); }
__device__ __forceinline__ float upb(unsigned lo) { return asf(lo << 10); }
template <bool EX> __device__ __forceinline__ f32x4 unpack4(v4u u) { return f32x4{upa<EX>(u.x, u.y), upb(u.x), upa<EX>(u.z, u.w), upb(u.z)}; }
__device__ __forceinline__ unsigned tagbad(v4u u, unsigned tsh) { return (u.y ^ tsh) | (u.w ^ tsh); }

template <int N> __device__ __forceinline__ void gissue(rsrc_t ws, const int (&off)[N], v4u (&u)[N]) {
  asm volatile("" ::: "memory");
#pragma unroll
  for (int i = 0; i < N; ++i) u[i] = ldh(ws, off[i]);
}
template <int N> __device__ __forceinline__ bool gok(const v4u (&u)[N], unsigned tsh) {
  unsigned bad = tagbad(u[0], tsh);
#pragma unroll
  for (int i = 1; i < N; ++i) bad |= tagbad(u[i], tsh);
  return __all(bad < 4096u);
}
template <int N, bool LIGHT> __device__ __forceinline__ void gretry(rsrc_t ws, const int (&off)[N], v4u (&u)[N], unsigned tsh) {
  if (LIGHT) {
    const int soff = __builtin_amdgcn_readfirstlane(off[N - 1]);
#pragma unroll 1
    for (unsigned spins = 0; spins < SPIN_CAP; ++spins) {
      asm volatile("" ::: "memory");
      const unsigned t = __builtin_amdgcn_readfirstlane(__builtin_amdgcn_raw_buffer_load_b32(ws, 12, soff, AUX_SC1));
      if ((t ^ tsh) < 4096u) break;
      __builtin_amdgcn_s_sleep(2);
    }
  }
#pragma unroll 1
  for (unsigned spins = 0; spins < SPIN_CAP; ++spins) {
    gissue<N>(ws, off, u);
    if (gok<N>(u, tsh)) break;
    if (RSLEEP) __builtin_amdgcn_s_sleep(RSLEEP);
  }
}
template <int K> __device__ __forceinline__ void upstream(rsrc_t ws, int soff, unsigned tsh) {
  unsigned spins = 0;
#pragma unroll 1
  for (; spins < SPIN_CAP; ++spins) {
    asm volatile("" ::: "memory");
    const unsigned t = __builtin_amdgcn_readfirstlane(__builtin_amdgcn_raw_buffer_load_b32(ws, 12, soff, AUX_SC1));
    if ((t ^ tsh) < 4096u) break;
    __builtin_amdgcn_s_sleep(1);
  }
  if (spins) __builtin_amdgcn_s_sleep(K);
}
template <int N, bool LIGHT, class F> __device__ __forceinline__ void gfinish(rsrc_t ws, const int (&off)[N], v4u (&u)[N], unsigned tsh, F f) {
  if (__builtin_expect(!gok<N>(u, tsh), 0)) gretry<N, LIGHT>(ws, off, u, tsh);
  f();
  asm volatile("" ::: "memory");
}
template <int M> __device__ __forceinline__ v4u diag(unsigned xx, v4u mask) { const unsigned t = dppu<BCAST + M>(xx); return v4u{t & mask.x, t & mask.y, t & mask.z, t & mask.w}; }
__device__ __forceinline__ v4u msk(unsigned d, v4u mask) { return v4u{d & mask.x, d & mask.y, d & mask.z, d & mask.w}; }
__device__ __forceinline__ F2 red_pair(f32x4 v) {
  const F2 p = sw32(v[0], v[2]), q = sw32(v[1], v[3]);
  float s0 = tr16(p.a, q.a), s1 = tr16(p.b, q.b);
  s0 += dpp<ROR8>(s0); s1 += dpp<ROR8>(s1);
  return {s0, s1};
}
__device__ __forceinline__ float red8(f32x4 v) {
  float s = tr16(tr32(v[0], v[2]), tr32(v[1], v[3]));
  s += dpp<ROR8>(s);
  return s;
}
template <class T> __device__ __forceinline__ LAS T* lp(LAS unsigned char* L, int off) { return (LAS T*)(L + off); }

#define STAMP(i) do {} while (0)
#define FLUSH() do {} while (0)

__device__ __forceinline__ void final32(LAS unsigned char* L, int rd, int pitch, rsrc_t ws, int dst, unsigned tag) {
  const f32x4 s0 = *lp<f32x4>(L, rd), s1 = *lp<f32x4>(L, rd + pitch), s2 = *lp<f32x4>(L, rd + 2 * pitch), s3 = *lp<f32x4>(L, rd + 3 * pitch);
  const f32x4 s = (s0 + s1) + (s2 + s3);
  const F2 p = sw32(s[0], s[2]), q = sw32(s[1], s[3]);
  sth2(ws, dst, pack2<true>(add1(p.a, p.b), add1(q.a, q.b), tag));
}

#define CTX()                                                                                                              \
  const int lane = tid & 63, xcd = b & 7, j = b >> 4;                                                                       \
  const int P = xcd >> 1, pj = (xcd & 1) * 16 + j;                                                                          \
  const int c16 = lane & 15, h = lane >> 4, c8 = lane & 7, u5 = lane & 31, hf = lane >> 5;                                  \
  const bool isR = xcd >= 4 && j >= 12;                                                                                     \
  const int ef = xcd < 4 ? xcd * 16 + j : 16 + xcd * 12 + j;                      \
  const int gr0 = 128 * xcd + 64 * (j & 1) + 8 * w + 2 * h;                                 \
  const rsrc_t ws = mk(ka->ws, WS_END), rwm = mk(ka->wm, D * 6144 * 2), rc = mk(ka->c, D * 2), rx = mk(ka->x, D * 2);               \
  SB();                                                                                                                     \
  if (PRIO && w == 0) __builtin_amdgcn_s_setprio(PRIO);                                                                                \
  v4u dmask;                                                                                                                \
  {                                                                                                                         \
    const unsigned mv = c16 >= 8 ? 0u : (c16 & 1) ? 0xFFFF0000u : 0x0000FFFFu;                                              \
    const int t = c8 >> 1;                                                                                                  \
    dmask = v4u{t == 0 ? mv : 0u, t == 1 ? mv : 0u, t == 2 ? mv : 0u, t == 3 ? mv : 0u};                                    \
  }
#define TAG_PUT() do { if (tid == 448) *lp<unsigned>(L, L_TAG) = (ep >> 12) + 1u & 0xFFFFFu; } while (0)
#define TAG_GET() const unsigned tsh = (tag = __builtin_amdgcn_readfirstlane(*lp<unsigned>(L, L_TAG))) << 12

__global__ void __launch_bounds__(512, 2) fused_block(Args) {
  __shared__ __attribute__((aligned(16))) unsigned char smem[LDS_BYTES];
  LAS unsigned char* L = (LAS unsigned char*)smem;
  typedef unsigned long long u64x4 __attribute__((ext_vector_type(4), aligned(8)));
  const CAS Args* ka = (const CAS Args*)__builtin_amdgcn_kernarg_segment_ptr();
  int b = blockIdx.x, tid = threadIdx.x;
  const int w = __builtin_amdgcn_readfirstlane(tid >> 6);
  STAMP(0);
  unsigned tag, gd0 = 0, gd1 = 0, pf = 0;
  if (CODE_BYTES && !(b & 8)) {
    unsigned long long code0; asm volatile("s_getpc_b64 %0" : "=s"(code0));
    if (tid >= 496) pf = __builtin_amdgcn_raw_buffer_load_b32(mk((const void*)code0, CODE_BYTES), (b >> 4) * 1024 + (tid - 496) * 64, 0, 0);
  }

  if (__builtin_expect(b & 8, 1)) {
    v4u aw[4], wv[8];
    v2u cv;
    unsigned ep;
    int va, vq;
    {
      u64x4 p4;
      asm volatile("s_load_dwordx8 %0, %1, 0x0" : "=&s"(p4) : "s"(ka));
      SB();
      const int r0 = 128 * (b & 7) + 16 * w + ((tid >> 2) & 12), cb = (tid & 15) * 16;
      int so = r0 * 2;
      va = r0 * 12288 + ((b >> 4) ^ CX) * 256 + cb;
      vq = (128 * (b & 6) + 32 * w + ((tid >> 4) & 3)) * 8192 + ((b & 1) * 16 + (b >> 4)) * 256 + cb;
      asm volatile("s_waitcnt lgkmcnt(0)" : "+v"(va), "+v"(so), "+v"(vq), "+s"(p4));
      SB();
      const rsrc_t rwm = mk((const void*)p4.y, D * 6144 * 2), rc = mk((const void*)p4.x, D * 2), rq = mk((const void*)p4.z, 8388608);
      ep = __builtin_amdgcn_raw_buffer_load_b32(mk((const void*)p4.w, w == 7 ? 8 : 0), WS_EPOCH + 4, 0, AUX_SC1);
      cv = __builtin_amdgcn_raw_buffer_load_b64(rc, so, 0, 0);
#pragma unroll
      for (int m = 0; m < 4; ++m) aw[m] = ldw(rwm, va, m * 12288);
      IBAR();
#pragma unroll
      for (int m = 0; m < BEARLY; ++m) wv[m] = ldw(rq, vq, m * 32768);
      SB();
    }
    asm volatile("" : "+s"(b), "+v"(tid), "+s"(ka));
    CTX();
    const v4u S0 = msk(dup16(silu(blo(cv.x))), dmask), S1 = msk(dup16(silu(bhi(cv.x))), dmask), S2 = msk(dup16(silu(blo(cv.y))), dmask), S3 = msk(dup16(silu(bhi(cv.y))), dmask);
    const int kb = P;
    int ard = L_RED1 + hf * 2048 + u5 * 16, adst = WS_H1 + (xcd * 16 + (j ^ CX)) * 512 + u5 * 16 + hf * 8;
    {
      int wr = L_RED1 + w * 512 + (2 * c16 + h) * 16;
      PIN(wr); PIN(ard); PIN(adst);
      f32x4 acc = mfma(S0, aw[0], f32x4{0, 0, 0, 0});
      acc = mfma(S1, aw[1], acc); acc = mfma(S2, aw[2], acc); acc = mfma(S3, aw[3], acc);
      if (lane < 32) *lp<f32x4>(L, wr) = acc;
      TAG_PUT();
      bar();
    }
    if (w == 0) final32(L, ard, 512, ws, adst, *lp<unsigned>(L, L_TAG));
    STAMP(10);
    IBAR();
    TAG_GET();
    {
      const rsrc_t rq = mk(ka->wq, 8388608);
      const v4u s0v = __builtin_amdgcn_raw_buffer_load_b128(rx, lane * 32, 0, 0), s1v = __builtin_amdgcn_raw_buffer_load_b128(rx, lane * 32 + 16, 0, 0);
      const unsigned xe = __builtin_amdgcn_raw_buffer_load_b16(rx, (256 * kb + 32 * w + 4 * c8 + h) * 2, 0, 0);
      const unsigned gcv = __builtin_amdgcn_raw_buffer_load_b32(rc, gr0 * 2, 0, 0);
#pragma unroll
      for (int m = BEARLY; m < 8; ++m) wv[m] = ldw(rq, vq, m * 32768);
      SB();
      float xv[16];
#pragma unroll
      for (int i = 0; i < 4; ++i) { xv[2 * i] = blo(s0v[i]); xv[2 * i + 1] = bhi(s0v[i]); xv[8 + 2 * i] = blo(s1v[i]); xv[9 + 2 * i] = bhi(s1v[i]); }
      float s = 0.f;
#pragma unroll
      for (int i = 0; i < 16; ++i) s += xv[i];
      const float mean = sum64(s) * (1.f / D);
      float q2 = 0.f;
#pragma unroll
      for (int i = 0; i < 16; ++i) { const float d = xv[i] - mean; q2 += d * d; }
      const float sdv = rbf(__builtin_sqrtf(rbf(rbf(sum64(q2) * (1.f / D)) + 1e-5f)));
      float nrm = rbf(rbf(blo(xe) - rbf(mean)) / sdv);
      gd0 = dup16(silu(blo(gcv))); gd1 = dup16(silu(bhi(gcv)));
      int wr = L_RED0 + w * 512 + (2 * c16 + h) * 16, rd = L_RED0 + hf * 2048 + u5 * 16, dst = WS_H2 + (pj * 4 + kb) * 512 + u5 * 16 + hf * 8;
      int off[2];
      off[0] = WS_H1 + (((lane >> 3) & 3) * 16 + hf * 8 + 2 * kb + (w >> 2)) * 512 + (w & 3) * 128 + c8 * 16; off[1] = off[0] + 32768;
      PIN(wr); PIN(rd); PIN(dst); PIN(nrm); PIN(off[0]); PIN(off[1]); PIN(gd0); PIN(gd1);
      asm volatile("s_waitcnt vmcnt(1)" ::: "memory");
      STAMP(1);
      v4u u[2]; gissue<2>(ws, off, u);
      unsigned xx;
      gfinish<2, L1>(ws, off, u, tsh, [&]() {
        const F2 sc = red_pair(unpack4<true>(u[0]) + unpack4<true>(u[1]));
        const unsigned pk = cvtpk(sc.a, sc.b);
        xx = dup16(rbf(nrm * rbf(1.f + bhi(pk))) + blo(pk));
        PIN(xx);
      });
      STAMP(2);
      f32x4 acc = {0, 0, 0, 0};
#define GST(M) acc = mfma(diag<M>(xx, dmask), wv[M], acc);
      GST(0) GST(1) GST(2) GST(3) GST(4) GST(5) GST(6) GST(7)
#undef GST
      if (lane < 32) *lp<f32x4>(L, wr) = acc;
      bar();
      if (w == 0) { final32(L, rd, 512, ws, dst, tag); DRAIN(); }
      STAMP(3);
      IBAR();
    }
    const int cg = ef % 14, ks = ef / 14;
    const rsrc_t rg = mk(ka->wg, isR ? 0 : D * FF * 2), ru = mk(ka->wu, isR ? 0 : D * FF * 2);
    v4u ew[16];
    const int vo = (128 * ks + 16 * w + h) * (FF * 2) + cg * 512 + c16 * 16, vu = vo ^ 512;
    {
      v4u gw[2], sw[4];
      const int vg = gr0 * 12288 + 4096 + (j >> 1) * 256 + c16 * 16;
      gw[0] = ldw(rwm, vg, 0); gw[1] = ldw(rwm, vg, 12288);
#pragma unroll
      for (int m = 0; m < 4; ++m) sw[m] = ldw(rwm, va + 6144, m * 12288);
      IBAR();
      ew[0] = ldw(rg, vo, 0); ew[1] = ldw(ru, vu, 0); ew[2] = ldw(rg, vo + 256, 0); ew[3] = ldw(ru, vu + 256, 0);
      SB();
      f32x4 ag = mfma(msk(gd0, dmask), gw[0], f32x4{0, 0, 0, 0}), as = mfma(S0, sw[0], f32x4{0, 0, 0, 0});
      ag = mfma(msk(gd1, dmask), gw[1], ag);
      as = mfma(S1, sw[1], as); as = mfma(S2, sw[2], as); as = mfma(S3, sw[3], as);
      if (lane < 32) { *lp<f32x4>(L, L_RED2 + w * 1024 + (2 * c16 + h) * 16) = ag; *lp<f32x4>(L, L_RED2 + w * 1024 + 512 + (2 * c16 + h) * 16) = as; }
      bar();
      if (w < 2) final32(L, L_RED2 + hf * 4096 + w * 512 + u5 * 16, 1024, ws, (w ? WS_HSC + (xcd * 16 + (j ^ CX)) * 512 : WS_HGA + ((2 * xcd + (j & 1)) * 8 + (j >> 1)) * 512) + u5 * 16 + hf * 8, tag);
      STAMP(4);
      IBAR();
    }
    if (!isR) {
#pragma unroll
      for (int m = 1; m < 4; ++m) {
        ew[4 * m] = ldw(rg, vo, m * 4 * FF * 2); ew[4 * m + 1] = ldw(ru, vu, m * 4 * FF * 2);
        ew[4 * m + 2] = ldw(rg, vo + 256, m * 4 * FF * 2); ew[4 * m + 3] = ldw(ru, vu + 256, m * 4 * FF * 2);
      }
      SB();
      int wr = L_V + w * 2048 + (2 * c16 + h) * 16, rd = L_V + hf * 8192 + (w & 3) * 512 + u5 * 16;
      int dst = WS_H5 + (ks * 112 + ((cg ^ (w & 1)) * 2 + ((w >> 1) & 1)) * 4 + (u5 >> 3)) * 256 + (w & 1) * 128 + c8 * 16 + hf * 8;
      const int ln = 4 * ks + (w >> 1), un = ((w & 1) * 4 + (lane & 3)) * 16;
      int offa[2], offb[2];
      offa[0] = WS_HSC + ((lane >> 3) * 16 + (ln >> 2)) * 512 + (ln & 3) * 128 + un; offa[1] = offa[0] + 4096;
      offb[0] = (ALLSTAT || w == 0) ? WS_H4B + u5 * 256 + 128 : WS_H4B + ln * 256 + un; offb[1] = WS_H4B + ln * 256 + un;
      PIN(wr); PIN(rd); PIN(dst); PIN(offa[0]); PIN(offa[1]); PIN(offb[0]); PIN(offb[1]);
      asm volatile("s_waitcnt vmcnt(2)" ::: "memory");
      STAMP(6);
      v4u ua[2], ub[2];
      gissue<2>(ws, offa, ua); gissue<2>(ws, offb, ub);
      float sb, c1;
      if (__builtin_expect(!gok<2>(ua, tsh), 0)) { gretry<2, true>(ws, offa, ua, tsh); gissue<2>(ws, offb, ub); }
      sb = rbf(red8(unpack4<true>(ua[0]))); c1 = rbf(1.f + rbf(red8(unpack4<true>(ua[1]))));
      PIN(sb); PIN(c1);
      STAMP(7);
      if (U4B && !gok<2>(ub, tsh)) { upstream<U4B>(ws, WS_H4A + (ef & 31) * 4096 + w * 512, tsh); gissue<2>(ws, offb, ub); }
      float x1, mb, rs;
      gfinish<2, L4B>(ws, offb, ub, tsh, [&]() {
        const unsigned l0 = (h & 2) ? ub[1].z : ub[1].x, h0 = (h & 2) ? ub[1].w : ub[1].y;
        x1 = (h & 1) ? upb(l0) : upa<true>(l0, h0);
        const float mean = sum32(asf(ub[0].x)) * (1.f / D), ex2 = sum32(asf(ub[0].z)) * (1.f / D);
        mb = rbf(mean);
        rs = __builtin_amdgcn_rcpf(rbf(__builtin_amdgcn_sqrtf(rbf(rbf(__builtin_fmaxf(ex2 - mean * mean, 0.f)) + 1e-5f))));
        PIN(x1); PIN(mb); PIN(rs);
      });
      STAMP(8);
      if (!ALLSTAT) {
        if (tid == 0) *lp<f32x2>(L, L_ST) = f32x2{mb, rs};
        bar();
        const f32x2 stv = *lp<f32x2>(L, L_ST);
        mb = stv[0]; rs = stv[1];
      }
      const unsigned xx = dup16(rbf(rbf(rbf(x1 - mb) * rs) * c1) + sb);
      f32x4 ac[4] = {{0, 0, 0, 0}, {0, 0, 0, 0}, {0, 0, 0, 0}, {0, 0, 0, 0}};
#define EST(M) { const v4u A = diag<M>(xx, dmask); ac[0] = mfma(A, ew[4 * M], ac[0]); ac[1] = mfma(A, ew[4 * M + 1], ac[1]); ac[2] = mfma(A, ew[4 * M + 2], ac[2]); ac[3] = mfma(A, ew[4 * M + 3], ac[3]); }
      EST(0) EST(1) EST(2) EST(3)
#undef EST
      if (lane < 32) { *lp<f32x4>(L, wr) = ac[0]; *lp<f32x4>(L, wr + 512) = ac[1]; *lp<f32x4>(L, wr + 1024) = ac[2]; *lp<f32x4>(L, wr + 1536) = ac[3]; }
      bar();
      if (w < 4) final32(L, rd, 2048, ws, dst, tag);
      STAMP(9);
      FLUSH();
      return;
    }
  } else {
    asm volatile("" : "+s"(b), "+v"(tid), "+s"(ka));
    CTX();
    const int g = P, gd = (P + 1) & 3, wd = pj >> 2;
#define DST(M, W) { const v4u A = diag<M>(xx, dmask); acc = mfma(A, W[2 * M], acc); acd = mfma(A, W[2 * M + 1], acd); }
    {
      v4u vv[4], kk[4], kf[4], vf[4], dw[8];
      const int vkv = (g * SK + SK - WIN + pj * 128) * 256 + w * 4096 + lane * 16;
      const rsrc_t rk = mk(ka->kc, 67108864), rv = mk(ka->vc, 67108864), ro = mk(ka->wo, 4096 * D * 2);
      const int vod = ((gd * 8 + w) * 128 + 16 * wd + h) * 2048 + (pj & 3) * 512 + c16 * 16;
      if (X_DELAY) __builtin_amdgcn_s_sleep(X_DELAY);
      const unsigned ep = __builtin_amdgcn_raw_buffer_load_b32(mk(ka->ws, w == 7 ? 8 : 0), WS_EPOCH + 4, 0, AUX_SC1);
      const unsigned gcv = __builtin_amdgcn_raw_buffer_load_b32(rc, gr0 * 2, 0, 0);
#pragma unroll
      for (int t = 0; t < 4; ++t) vv[t] = ldw(rv, vkv, t * 1024);
#pragma unroll
      for (int t = 0; t < 4; ++t) kk[t] = ldw(rk, vkv, t * 1024);
      IBAR();
#pragma unroll
      for (int m = 0; m < 8; ++m) dw[m] = ldw(ro, vod + (m & 1) * 256, (m >> 1) * 8192);
      SB();
      if (lane < 8) lp<float>(L, L_WMAX)[lane] = -__builtin_inff();
      asm volatile("" :: "v"(pf));
      gd0 = dup16(silu(blo(gcv))); gd1 = dup16(silu(bhi(gcv)));
#pragma unroll
      for (int t = 0; t < 4; ++t) *lp<v4u>(L, L_V + (16 * w + 4 * t + h) * 256 + 16 * (c16 ^ ((h << 2) | t))) = vv[t];
#pragma unroll
      for (int t = 0; t < 4; ++t) *lp<v4u>(L, L_K + w * 4096 + (4 * t + h) * 256 + 16 * (c16 ^ (4 * t + h))) = kk[t];
      TAG_PUT();
      bar();
      TAG_GET();
      {
        const int q = c16 >> 2, p = c16 & 3, ch = 2 * w + (p >> 1);
        const unsigned base = (unsigned)(size_t)L + L_V + (8 * h + q) * 256 + 8 * (p & 1);
        const unsigned a0 = base + 16 * (ch ^ ((q << 2) | ((2 * h) & 3))), a1 = base + 1024 + 16 * (ch ^ ((q << 2) | ((2 * h + 1) & 3)));
#define VTR(S) { v2u lo, hi; asm volatile("ds_read_b64_tr_b16 %0, %2 offset:%4\n\tds_read_b64_tr_b16 %1, %3 offset:%4\n\ts_waitcnt lgkmcnt(0)" : "=&v"(lo), "=&v"(hi) : "v"(a0), "v"(a1), "i"(S * 8192) : "memory"); vf[S] = v4u{lo.x, lo.y, hi.x, hi.y}; }
        VTR(0) VTR(1) VTR(2) VTR(3)
#undef VTR
      }
#pragma unroll
      for (int s = 0; s < 4; ++s) kf[s] = *lp<v4u>(L, L_K + w * 4096 + c16 * 256 + 16 * ((4 * s + h) ^ c16));
      asm volatile("" : "+v"(kf[0]), "+v"(kf[1]), "+v"(kf[2]), "+v"(kf[3]));
      int dwr = L_RED2 + w * 1024 + (2 * c16 + h) * 16, drd = L_RED2 + hf * 4096 + (w & 1) * 512 + u5 * 16;
      int ddst = WS_H4A + (gd * 8 + wd) * 4096 + (pj & 3) * 1024 + (w & 1) * 512 + u5 * 16 + hf * 8;
      int doff[3];
      doff[0] = WS_H3D + ((gd * 32 + (lane >> 2)) * 32 + wd * 4 + (w >> 1)) * 128 + ((w & 1) * 4 + (lane & 3)) * 16; doff[1] = doff[0] + 65536;
      doff[2] = WS_H3S + (gd * 32 + 16 * (lane & 1) + (lane >> 2)) * 128 + (w >> 1) * 16;
      PIN(dwr); PIN(drd); PIN(ddst); PIN(doff[0]); PIN(doff[1]); PIN(doff[2]); PIN(gd0); PIN(gd1);
      {
        int qw = L_Q + w * 272 + u5 * 8 + hf * 4, fr = c8 * 272 + h * 16, mw = L_WMAX + c8 * 4, pw = L_P + c8 * 272 + w * 32 + h * 8;
        int od = WS_H3D + ((g * 32 + pj) * 32 + w * 4 + (c8 >> 1)) * 128 + ((c16 & 1) * 4 + h) * 16, sd = WS_H3S + (g * 32 + pj) * 128 + c8 * 8;
        int off[2] = {WS_H2 + ((g * 8 + w) * 4 + hf) * 512 + u5 * 16, WS_H2 + ((g * 8 + w) * 4 + 2 + hf) * 512 + u5 * 16};
        PIN(qw); PIN(fr); PIN(mw); PIN(pw); PIN(od); PIN(sd); PIN(off[0]); PIN(off[1]);
        asm volatile("s_waitcnt vmcnt(1)" ::: "memory");
        STAMP(3);
        v4u u[2]; gissue<2>(ws, off, u);
        unsigned qq;
        gfinish<2, L2>(ws, off, u, tsh, [&]() {
          const f32x4 qs = unpack4<true>(u[0]) + unpack4<true>(u[1]);
          qq = cvtpk(tr32(qs[0], qs[2]), tr32(qs[1], qs[3]));
          PIN(qq);
        });
        STAMP(4);
        *lp<unsigned>(L, qw) = qq;
        bar();
        const v4u q0 = *lp<v4u>(L, L_Q + fr), q1 = *lp<v4u>(L, L_Q + fr + 64), q2 = *lp<v4u>(L, L_Q + fr + 128), q3 = *lp<v4u>(L, L_Q + fr + 192);
        SB();
        f32x4 sc = mfma(kf[0], q0, f32x4{0, 0, 0, 0});
        sc = mfma(kf[1], q1, sc); sc = mfma(kf[2], q2, sc); sc = mfma(kf[3], q3, sc);
        {
          const unsigned t0 = cvtpk(sc[0], sc[1]), t1 = cvtpk(sc[2], sc[3]);
          constexpr float SCL = 0.08838834764831845f;
          const unsigned r0 = cvtpk(blo(t0) * SCL, bhi(t0) * SCL), r1 = cvtpk(blo(t1) * SCL, bhi(t1) * SCL);
          sc = f32x4{blo(r0), bhi(r0), blo(r1), bhi(r1)};
        }
        const float mx = max3(max3(sc[0], sc[1], sc[2]), sc[3], sc[3]);
        if (c16 < 8) __hip_atomic_fetch_max(lp<float>(L, mw), mx, __ATOMIC_RELAXED, __HIP_MEMORY_SCOPE_WORKGROUP);
        bar();
        const float m = *lp<float>(L, mw);
        const float ml = m * LOG2E;
        const float p0 = __builtin_amdgcn_exp2f(sc[0] * LOG2E - ml), p1 = __builtin_amdgcn_exp2f(sc[1] * LOG2E - ml), p2 = __builtin_amdgcn_exp2f(sc[2] * LOG2E - ml), p3 = __builtin_amdgcn_exp2f(sc[3] * LOG2E - ml);
        if (c16 < 8) *lp<v2u>(L, pw) = v2u{cvtpk(p0, p1), cvtpk(p2, p3)};
        bar();
        const v4u b0 = *lp<v4u>(L, L_P + fr), b1 = *lp<v4u>(L, L_P + fr + 64), b2 = *lp<v4u>(L, L_P + fr + 128), b3 = *lp<v4u>(L, L_P + fr + 192);
        SB();
        f32x4 o = {0, 0, 0, 0};
        if (w == 0) {
          const v4u one = {0x3F803F80u, 0x3F803F80u, 0x3F803F80u, 0x3F803F80u};
          f32x4 l = mfma(one, b0, f32x4{0, 0, 0, 0}); o = mfma(vf[0], b0, o);
          l = mfma(one, b1, l); o = mfma(vf[1], b1, o); l = mfma(one, b2, l); o = mfma(vf[2], b2, o); l = mfma(one, b3, l); o = mfma(vf[3], b3, o);
          if (lane < 8) sth2(ws, sd, pack2<false>(l[0], m, tag));
        } else {
          o = mfma(vf[0], b0, o); o = mfma(vf[1], b1, o); o = mfma(vf[2], b2, o); o = mfma(vf[3], b3, o);
        }
        if (c16 < 8) sth(ws, od, pack4(o, tag));
        STAMP(5);
      }
      __builtin_amdgcn_s_sleep(H3WAIT);
      v4u u[3]; gissue<3>(ws, doff, u);
      unsigned xx;
      gfinish<3, false>(ws, doff, u, tsh, [&]() {
        const unsigned slo = (w & 1) ? u[2].z : u[2].x, shi = (w & 1) ? u[2].w : u[2].y;
        const float m = upb(slo), l = upa<false>(slo, shi);
        const float wg = __builtin_amdgcn_exp2f((m - maxb(m)) * LOG2E);
        float Lt = wg * l; Lt += dpp<XOR1>(Lt); Lt += dpp<ROR8>(Lt); Lt += dpp<ROR4>(Lt); Lt = tr16(Lt, Lt); Lt = tr32(Lt, Lt);
        const f32x4 v = unpack4<false>(u[0]) * dpp<QB0>(wg) + unpack4<false>(u[1]) * dpp<QB1>(wg);
        float o = red8(v); o += dpp<ROR4>(o);
        xx = dup16(o * __builtin_amdgcn_rcpf(Lt));
        PIN(xx);
      });
      STAMP(6);
      f32x4 acc = {0, 0, 0, 0}, acd = {0, 0, 0, 0};
      DST(0, dw) DST(1, dw) DST(2, dw) DST(3, dw)
      if (lane < 32) { *lp<f32x4>(L, dwr) = acc; *lp<f32x4>(L, dwr + 512) = acd; }
      bar();
      if (w < 2) final32(L, drd, 1024, ws, ddst, tag);
      STAMP(7);
    }
    if (!isR) {
      const unsigned tsh = tag << 12;
      __builtin_amdgcn_s_sleep(F_DELAY);
      IBAR();
      v4u gw[2], wv[8];
      const int vg = gr0 * 12288 + 10240 + (j >> 1) * 256 + c16 * 16;
      gw[0] = ldw(rwm, vg, 0); gw[1] = ldw(rwm, vg, 12288);
      IBAR();
      const rsrc_t rd = mk(ka->wd, FF * D * 2);
      const int vo = (128 * (ef >> 2) + 16 * w + h) * 2048 + (ef & 3) * 512 + c16 * 16;
#pragma unroll
      for (int m = 0; m < 8; ++m) wv[m] = ldw(rd, vo + (m & 1) * 256, (m >> 1) * 8192);
      SB();
      {
        f32x4 ag = mfma(msk(gd0, dmask), gw[0], f32x4{0, 0, 0, 0});
        ag = mfma(msk(gd1, dmask), gw[1], ag);
        if (lane < 32) *lp<f32x4>(L, L_RED1 + w * 512 + (2 * c16 + h) * 16) = ag;
        bar();
        if (w == 0) final32(L, L_RED1 + hf * 2048 + u5 * 16, 512, ws, WS_HGB + ((2 * xcd + (j & 1)) * 8 + (j >> 1)) * 512 + u5 * 16 + hf * 8, tag);
      }
      int wr = L_V + w * 1024 + (2 * c16 + h) * 16, rr = L_V + hf * 4096 + (w & 1) * 512 + u5 * 16;
      int dst = WS_H6 + (ef >> 2) * 4096 + (ef & 3) * 1024 + (w & 1) * 512 + u5 * 16 + hf * 8;
      int off[1] = {WS_H5 + (((lane >> 2) & 7) * 112 + (ef & ~3) + (w >> 1)) * 256 + hf * 128 + ((w & 1) * 4 + (lane & 3)) * 16};
      PIN(wr); PIN(rr); PIN(dst); PIN(off[0]);
      asm volatile("s_waitcnt vmcnt(1)" ::: "memory");
      STAMP(8);
      if (U5) upstream<U5>(ws, WS_H4B + (ef & 31) * 256, tsh);
      v4u u[1]; gissue<1>(ws, off, u);
      unsigned xx;
      gfinish<1, L5 && !U5>(ws, off, u, tsh, [&]() {
        F2 gu = red_pair(unpack4<false>(u[0]));
        gu.a += dpp<ROR4>(gu.a); gu.b += dpp<ROR4>(gu.b);
        xx = dup16(silu(gu.a) * gu.b);
        PIN(xx);
      });
      STAMP(9);
      f32x4 acc = {0, 0, 0, 0}, acd = {0, 0, 0, 0};
      DST(0, wv) DST(1, wv) DST(2, wv) DST(3, wv)
      if (lane < 32) { *lp<f32x4>(L, wr) = acc; *lp<f32x4>(L, wr + 512) = acd; }
      bar();
      if (w < 2) final32(L, rr, 1024, ws, dst, tag);
      STAMP(10);
      FLUSH();
      return;
    }
#undef DST
  }
  {
    const int lane = tid & 63, xcd = b & 7, j = b >> 4, c16 = lane & 15, h = lane >> 4, c8 = lane & 7, u5 = lane & 31, hf = lane >> 5;
    const int r = (xcd - 4) * 8 + ((b & 8) >> 1) + (j - 12);
    const rsrc_t ws = mk(ka->ws, WS_END), rout = mk(ka->out, D * 2);
    const unsigned tsh = tag << 12;
    float x1 = 0.f;
    const int lo = (lane >> 3) * 4096 + r * 128 + c8 * 16;
    auto gatevec = [&](int base) {
      int off[2] = {base + ((lane >> 3) * 8 + (r >> 2)) * 512 + (r & 3) * 128 + c8 * 16, 0};
      off[1] = off[0] + 32768;
      v4u u[2]; gissue<2>(ws, off, u);
      if (!gok<2>(u, tsh)) gretry<2, true>(ws, off, u, tsh);
      return rbf(red8(unpack4<true>(u[0]) + unpack4<true>(u[1])));
    };
    int oo = (32 * r + 4 * c8 + h) * 2;
    if (w == 0) {
      const unsigned xe = __builtin_amdgcn_raw_buffer_load_b16(mk(ka->x, D * 2), oo, 0, 0);
      float ga = gatevec(WS_HGA);
      int off[4] = {WS_H4A + lo, WS_H4A + lo + 32768, WS_H4A + lo + 65536, WS_H4A + lo + 98304};
      int dst = WS_H4B + r * 256 + ((lane & 8) ? 128 + c8 * 8 : c8 * 16 + hf * 8);
      float xf = blo(xe);
      PIN(off[0]); PIN(off[1]); PIN(off[2]); PIN(off[3]); PIN(dst); PIN(ga); PIN(xf); PIN(oo);
      if (U4A && (b & 8)) upstream<U4A>(ws, WS_H3S + r * 512, tsh);
      v4u u[4]; gissue<4>(ws, off, u);
      v2u gq;
      gfinish<4, LR>(ws, off, u, tsh, [&]() {
        const float at = red8((unpack4<false>(u[0]) + unpack4<false>(u[1])) + (unpack4<false>(u[2]) + unpack4<false>(u[3])));
        x1 = rbf(xf + rbf(ga * rbf(at)));
        PIN(x1);
      });
      STAMP(11);
      float s1 = sum8(x1), s2 = sum8(x1 * x1);
      s1 = tr16(s1, s1); s2 = tr16(s2, s2);
      const float ss = tr32(s1, s1), sq = tr32(s2, s2);
      const F2 pr = sw16(x1, x1);
      gq = pack2<false>(pr.a, pr.b, tag);
      if (lane & 8) gq = v2u{asu(c8 ? sq : ss), tsh};
      if ((lane & 24) == 0 || lane == 8 || lane == 9) sth2(ws, dst, gq);
      if (OUT0 && (lane & 24) == 0) __builtin_amdgcn_raw_buffer_store_b32(0u, rout, oo, 0, 0);
      STAMP(12);
      DRAIN();
    }
    if (!(b & 8)) {
      IBAR();
      const rsrc_t rwm = mk(ka->wm, D * 6144 * 2);
      const int vg = (128 * xcd + 64 * (j & 1) + 8 * w + 2 * h) * 12288 + 10240 + (j >> 1) * 256 + c16 * 16;
      const v4u g0 = ldw(rwm, vg, 0), g1 = ldw(rwm, vg, 12288);
      const unsigned mv = c16 >= 8 ? 0u : (c16 & 1) ? 0xFFFF0000u : 0x0000FFFFu;
      const int t = c8 >> 1;
      const v4u dmask = {t == 0 ? mv : 0u, t == 1 ? mv : 0u, t == 2 ? mv : 0u, t == 3 ? mv : 0u};
      f32x4 ag = mfma(msk(gd0, dmask), g0, f32x4{0, 0, 0, 0});
      ag = mfma(msk(gd1, dmask), g1, ag);
      if (lane < 32) *lp<f32x4>(L, L_RED1 + w * 512 + (2 * c16 + h) * 16) = ag;
      bar();
      if (w == 0) final32(L, L_RED1 + hf * 2048 + u5 * 16, 512, ws, WS_HGB + ((2 * xcd + (j & 1)) * 8 + (j >> 1)) * 512 + u5 * 16 + hf * 8, tag);
    }
    if (w == 0) {
      float gb = gatevec(WS_HGB);
      int off[4] = {WS_H6 + lo, WS_H6 + lo + 32768, WS_H6 + lo + 65536, WS_H6 + (lo & ~0x4000) + 98304};
      float keep = lane < 32 ? 1.f : 0.f;
      PIN(off[0]); PIN(off[1]); PIN(off[2]); PIN(off[3]); PIN(gb); PIN(keep);
      if (U6) upstream<U6>(ws, WS_H5 + ((r & 7) * 112 + r * 3) * 256, tsh);
      v4u u[4]; gissue<4>(ws, off, u);
      unsigned res;
      gfinish<4, LR>(ws, off, u, tsh, [&]() {
        const float y = red8((unpack4<false>(u[0]) + unpack4<false>(u[1])) + (unpack4<false>(u[2]) + unpack4<false>(u[3]) * keep));
        const float o = x1 + rbf(gb * rbf(y));
        const F2 pr = sw16(o, o);
        res = cvtpk(pr.a, pr.b);
        PIN(res);
      });
      STAMP(13);
      if ((lane & 24) == 0) __builtin_amdgcn_raw_buffer_store_b32(res, rout, oo, 0, 0);
      if (r == 0 && lane == 0) __builtin_amdgcn_raw_buffer_store_b32(tsh, ws, WS_EPOCH + 4, 0, AUX_SC1);
      STAMP(14);
    }
    FLUSH();
  }
}
}

extern "C" void kernel_launch(void* const* d_in, const int* in_sizes, int n_in, void* d_out, int out_size, void* d_ws, size_t ws_size, hipStream_t stream) {
  Args a;
  a.x = (const u16*)d_in[0]; a.kc = (const u16*)d_in[1]; a.vc = (const u16*)d_in[2]; a.wq = (const u16*)d_in[3]; a.wo = (const u16*)d_in[4];
  a.wg = (const u16*)d_in[6]; a.wu = (const u16*)d_in[7]; a.wd = (const u16*)d_in[8]; a.c = (const u16*)d_in[9]; a.wm = (const u16*)d_in[10];
  a.out = (u16*)d_out; a.ws = (unsigned char*)d_ws;
  hipLaunchKernelGGL(fused_block, dim3(256), dim3(512), 0, stream, a);
}
```

```cpp
#include <hip/hip_runtime.h>
#include <hip/hip_bf16.h>

#define PROF 0
#define KNOB(n, v)
#define X_DELAY 22
#define H3WAIT 25
#define H4WAIT 0
#define F_DELAY 30
#define BEARLY 4
#define CODE_BYTES 14600
#define CX 2
#define SPEC 0
#define ALLSTAT 0

namespace {
typedef unsigned short u16;
typedef unsigned v4u __attribute__((ext_vector_type(4)));
typedef unsigned v2u __attribute__((ext_vector_type(2)));
typedef float f32x4 __attribute__((ext_vector_type(4)));
typedef float f32x2 __attribute__((ext_vector_type(2)));
typedef __bf16 bf16x8 __attribute__((ext_vector_type(8)));
typedef __bf16 bf16x2 __attribute__((ext_vector_type(2)));
typedef __amdgpu_buffer_rsrc_t rsrc_t;
#define LAS __attribute__((address_space(3)))
#define GAS __attribute__((address_space(1)))
#define CAS __attribute__((address_space(4)))

constexpr int D = 1024, SK = 65536, WIN = 4096, FF = 3584;
constexpr int AUX_NT = 2, AUX_SC1 = 16;
constexpr unsigned SPIN_CAP = 1u << 17;
constexpr float LOG2E = 1.4426950408889634f;

constexpr int WS_EPOCH = 0;
constexpr int WS_H1 = 4096;
constexpr int WS_H2 = WS_H1 + 65536;
constexpr int WS_H3S = WS_H2 + 65536;
constexpr int WS_HGA = WS_H3S + 16384;
constexpr int WS_HGB = WS_HGA + 65536;
constexpr int WS_HSC = WS_HGB + 65536;
constexpr int WS_H4B = WS_HSC + 65536;
constexpr int WS_H5 = WS_H4B + 8192;
constexpr int WS_H3D = WS_H5 + 229376;
constexpr int WS_H4A = WS_H3D + 524288;
constexpr int WS_H6 = WS_H4A + 131072;
constexpr int WS_END = WS_H6 + 114688;
constexpr int WS_PROF = 4 << 20;

constexpr int L_RED0 = 0;
constexpr int L_RED1 = L_RED0 + 4096;
constexpr int L_RED2 = L_RED1 + 4096;
constexpr int L_Q = L_RED2 + 8192;
constexpr int L_P = L_Q + 8 * 272;
constexpr int L_WMAX = L_P + 8 * 272;
constexpr int L_ST = L_WMAX + 32;
constexpr int L_TAG = L_ST + 8;
constexpr int L_V = 24576;
constexpr int L_K = L_V + 32768;
constexpr int LDS_BYTES = L_K + 32768;
static_assert(L_ST + 16 <= L_V && LDS_BYTES > 81920, "lds");

struct Args {
  const u16 *c, *wm, *wq;
  unsigned char* ws;
  const u16 *x, *kc, *vc, *wo, *wg, *wu, *wd;
  u16* out;
};

__device__ __forceinline__ rsrc_t mk(const void* p, unsigned bytes) { return __builtin_amdgcn_make_buffer_rsrc((void*)p, 0, bytes, 0x00020000); }
__device__ __forceinline__ v4u ldw(rsrc_t r, int voff, int soff) { return __builtin_amdgcn_raw_buffer_load_b128(r, voff, soff, AUX_NT); }
__device__ __forceinline__ v4u ldh(rsrc_t r, int voff) { return __builtin_amdgcn_raw_buffer_load_b128(r, voff, 0, AUX_SC1); }
__device__ __forceinline__ void sth(rsrc_t r, int voff, v4u v) { __builtin_amdgcn_raw_buffer_store_b128(v, r, voff, 0, AUX_SC1); }
__device__ __forceinline__ void sth2(rsrc_t r, int voff, v2u v) { __builtin_amdgcn_raw_buffer_store_b64(v, r, voff, 0, AUX_SC1); }
__device__ __forceinline__ float asf(unsigned u) { return __builtin_bit_cast(float, u); }
__device__ __forceinline__ unsigned asu(float f) { return __builtin_bit_cast(unsigned, f); }
__device__ __forceinline__ unsigned cvtpk(float lo, float hi) { return __builtin_bit_cast(unsigned, __builtin_convertvector(f32x2{lo, hi}, bf16x2)); }
__device__ __forceinline__ unsigned dup16(float f) { return cvtpk(f, f); }
__device__ __forceinline__ float blo(unsigned p) { return asf(p << 16); }
__device__ __forceinline__ float bhi(unsigned p) { return asf(p & 0xFFFF0000u); }
__device__ __forceinline__ float rbf(float f) { return bhi(cvtpk(f, f)); }
__device__ __forceinline__ float silu(float x) { return x * __builtin_amdgcn_rcpf(1.f + __builtin_amdgcn_exp2f(x * -LOG2E)); }
__device__ __forceinline__ float max3(float a, float b, float c) { float r; asm("v_max3_f32 %0, %1, %2, %3" : "=v"(r) : "v"(a), "v"(b), "v"(c)); return r; }
__device__ __forceinline__ float add1(float a, float b) { float r; asm("v_add_f32 %0, %1, %2" : "=v"(r) : "v"(a), "v"(b)); return r; }
#define PIN(x) asm volatile("" : "+v"(x))
#define PINS(x) asm volatile("" : "+s"(x))
template <int C> __device__ __forceinline__ unsigned dppu(unsigned x) { return (unsigned)__builtin_amdgcn_mov_dpp((int)x, C, 0xf, 0xf, true); }
template <int C> __device__ __forceinline__ float dpp(float x) { return asf(dppu<C>(asu(x))); }
constexpr int ROR4 = 0x124, QB0 = 0x00, QB1 = 0x55, XOR1 = 0xB1, XOR2 = 0x4E, HMIR = 0x141, ROR8 = 0x128, BCAST = 0x150;
struct F2 { float a, b; };
__device__ __forceinline__ F2 sw32(float a, float b) { auto r = __builtin_amdgcn_permlane32_swap(asu(a), asu(b), false, false); return {asf(r[0]), asf(r[1])}; }
__device__ __forceinline__ F2 sw16(float a, float b) { auto r = __builtin_amdgcn_permlane16_swap(asu(a), asu(b), false, false); return {asf(r[0]), asf(r[1])}; }
__device__ __forceinline__ float tr32(float a, float b) { F2 r = sw32(a, b); return r.a + r.b; }
__device__ __forceinline__ float tr16(float a, float b) { F2 r = sw16(a, b); return r.a + r.b; }
__device__ __forceinline__ float sum8(float v) { v += dpp<XOR1>(v); v += dpp<XOR2>(v); v += dpp<HMIR>(v); return v; }
__device__ __forceinline__ float sum16(float v) { v = sum8(v); v += dpp<ROR8>(v); return v; }
__device__ __forceinline__ float sum32(float v) { v = sum16(v); return tr16(v, v); }
__device__ __forceinline__ float sum64(float v) { v = sum32(v); return tr32(v, v); }
__device__ __forceinline__ float maxb(float v) {
  float t;
  asm("s_nop 1\n\tv_max_f32_dpp %0, %0, %0 quad_perm:[1,0,3,2] row_mask:0xf bank_mask:0xf\n\t"
      "s_nop 1\n\tv_max_f32_dpp %0, %0, %0 row_ror:8 row_mask:0xf bank_mask:0xf\n\t"
      "s_nop 1\n\tv_max_f32_dpp %0, %0, %0 row_ror:4 row_mask:0xf bank_mask:0xf\n\t"
      "v_mov_b32 %1, %0\n\ts_nop 1\n\tv_permlane16_swap_b32 %0, %1\n\tv_max_f32 %0, %0, %1\n\t"
      "v_mov_b32 %1, %0\n\ts_nop 1\n\tv_permlane32_swap_b32 %0, %1\n\tv_max_f32 %0, %0, %1"
      : "+v"(v), "=&v"(t));
  return v;
}
__device__ __forceinline__ f32x4 mfma(v4u a, v4u b, f32x4 c) { return __builtin_amdgcn_mfma_f32_16x16x32_bf16(__builtin_bit_cast(bf16x8, a), __builtin_bit_cast(bf16x8, b), c, 0, 0, 0); }
__device__ __forceinline__ void bar() { asm volatile("s_waitcnt lgkmcnt(0)" ::: "memory"); __builtin_amdgcn_s_barrier(); asm volatile("" ::: "memory"); }
#define SB() __builtin_amdgcn_sched_barrier(0)
#define DRAIN() asm volatile("s_waitcnt vmcnt(0)" ::: "memory")
#define IBAR() do { SB(); __builtin_amdgcn_s_barrier(); SB(); } while (0)

template <bool RN> __device__ __forceinline__ v2u pack2(float a, float b, unsigned tag) {
  const unsigned ua = asu(a) + (RN ? 0x200u : 0u), ub = asu(b) + (RN ? 0x200u : 0u);
  return v2u{__builtin_amdgcn_alignbit(ua >> 10, ub, 10), __builtin_amdgcn_alignbit(tag, ua, 20)};
}
__device__ __forceinline__ v4u pack4(f32x4 v, unsigned tag) { const v2u p = pack2<false>(v[0], v[1], tag), q = pack2<false>(v[2], v[3], tag); return v4u{p.x, p.y, q.x, q.y}; }
template <bool EX> __device__ __forceinline__ float upa(unsigned lo, unsigned hi) { return asf(__builtin_amdgcn_alignbit(hi, lo, 12) & (EX ? 0xFFFFFC00u : 0xFFFFFFFFu)); }
__device__ __forceinline__ float upb(unsigned lo) { return asf(lo << 10); }
template <bool EX> __device__ __forceinline__ f32x4 unpack4(v4u u) { return f32x4{upa<EX>(u.x, u.y), upb(u.x), upa<EX>(u.z, u.w), upb(u.z)}; }
__device__ __forceinline__ unsigned tagbad(v4u u, unsigned tsh) { return (u.y ^ tsh) | (u.w ^ tsh); }

template <int N> __device__ __forceinline__ void gissue(rsrc_t ws, const int (&off)[N], v4u (&u)[N]) {
  asm volatile("" ::: "memory");
#pragma unroll
  for (int i = 0; i < N; ++i) u[i] = ldh(ws, off[i]);
}
template <int N> __device__ __forceinline__ bool gok(const v4u (&u)[N], unsigned tsh) {
  unsigned bad = tagbad(u[0], tsh);
#pragma unroll
  for (int i = 1; i < N; ++i) bad |= tagbad(u[i], tsh);
  return __all(bad < 4096u);
}
template <int N, bool LIGHT> __device__ __forceinline__ void gretry(rsrc_t ws, const int (&off)[N], v4u (&u)[N], unsigned tsh) {
  if (LIGHT) {
    const int soff = __builtin_amdgcn_readfirstlane(off[N - 1]);
#pragma unroll 1
    for (unsigned spins = 0; spins < SPIN_CAP; ++spins) {
      asm volatile("" ::: "memory");
      const unsigned t = __builtin_amdgcn_readfirstlane(__builtin_amdgcn_raw_buffer_load_b32(ws, 12, soff, AUX_SC1));
      if ((t ^ tsh) < 4096u) break;
      __builtin_amdgcn_s_sleep(2);
    }
  }
#pragma unroll 1
  for (unsigned spins = 0; spins < SPIN_CAP; ++spins) {
    gissue<N>(ws, off, u);
    if (gok<N>(u, tsh)) break;
    __builtin_amdgcn_s_sleep(1);
  }
}
template <int N, bool LIGHT, class F> __device__ __forceinline__ void gfinish(rsrc_t ws, const int (&off)[N], v4u (&u)[N], unsigned tsh, F f) {
  if (__builtin_expect(!gok<N>(u, tsh), 0)) gretry<N, LIGHT>(ws, off, u, tsh);
  f();
  asm volatile("" ::: "memory");
}
template <int M> __device__ __forceinline__ v4u diag(unsigned xx, v4u mask) { const unsigned t = dppu<BCAST + M>(xx); return v4u{t & mask.x, t & mask.y, t & mask.z, t & mask.w}; }
__device__ __forceinline__ v4u msk(unsigned d, v4u mask) { return v4u{d & mask.x, d & mask.y, d & mask.z, d & mask.w}; }
__device__ __forceinline__ F2 red_pair(f32x4 v) {
  const F2 p = sw32(v[0], v[2]), q = sw32(v[1], v[3]);
  float s0 = tr16(p.a, q.a), s1 = tr16(p.b, q.b);
  s0 += dpp<ROR8>(s0); s1 += dpp<ROR8>(s1);
  return {s0, s1};
}
__device__ __forceinline__ float red8(f32x4 v) {
  float s = tr16(tr32(v[0], v[2]), tr32(v[1], v[3]));
  s += dpp<ROR8>(s);
  return s;
}
template <class T> __device__ __forceinline__ LAS T* lp(LAS unsigned char* L, int off) { return (LAS T*)(L + off); }

#define STAMP(i) do {} while (0)
#define FLUSH() do {} while (0)

__device__ __forceinline__ void final32(LAS unsigned char* L, int rd, int pitch, rsrc_t ws, int dst, unsigned tag) {
  const f32x4 s0 = *lp<f32x4>(L, rd), s1 = *lp<f32x4>(L, rd + pitch), s2 = *lp<f32x4>(L, rd + 2 * pitch), s3 = *lp<f32x4>(L, rd + 3 * pitch);
  const f32x4 s = (s0 + s1) + (s2 + s3);
  const F2 p = sw32(s[0], s[2]), q = sw32(s[1], s[3]);
  sth2(ws, dst, pack2<true>(add1(p.a, p.b), add1(q.a, q.b), tag));
}

#define CTX()                                                                                                              \
  const int lane = tid & 63, xcd = b & 7, j = b >> 4;                                                                       \
  const int P = xcd >> 1, pj = (xcd & 1) * 16 + j;                                                                          \
  const int c16 = lane & 15, h = lane >> 4, c8 = lane & 7, u5 = lane & 31, hf = lane >> 5;                                  \
  const bool isR = xcd >= 4 && j >= 12;                                                                                     \
  const int ef = xcd < 4 ? xcd * 16 + j : 16 + xcd * 12 + j;                      \
  const int gr0 = 128 * xcd + 64 * (j & 1) + 8 * w + 2 * h;                                 \
  const rsrc_t ws = mk(ka->ws, WS_END), rwm = mk(ka->wm, D * 6144 * 2), rc = mk(ka->c, D * 2), rx = mk(ka->x, D * 2);               \
  SB();                                                                                                                     \
  if (w == 0) __builtin_amdgcn_s_setprio(2);                                                                                \
  v4u dmask;                                                                                                                \
  {                                                                                                                         \
    const unsigned mv = c16 >= 8 ? 0u : (c16 & 1) ? 0xFFFF0000u : 0x0000FFFFu;                                              \
    const int t = c8 >> 1;                                                                                                  \
    dmask = v4u{t == 0 ? mv : 0u, t == 1 ? mv : 0u, t == 2 ? mv : 0u, t == 3 ? mv : 0u};                                    \
  }
#define TAG_PUT() do { if (tid == 448) *lp<unsigned>(L, L_TAG) = (ep >> 12) + 1u & 0xFFFFFu; } while (0)
#define TAG_GET() const unsigned tsh = (tag = __builtin_amdgcn_readfirstlane(*lp<unsigned>(L, L_TAG))) << 12

__global__ void __launch_bounds__(512, 2) fused_block(Args) {
  __shared__ __attribute__((aligned(16))) unsigned char smem[LDS_BYTES];
  LAS unsigned char* L = (LAS unsigned char*)smem;
  typedef unsigned long long u64x4 __attribute__((ext_vector_type(4), aligned(8)));
  const CAS Args* ka = (const CAS Args*)__builtin_amdgcn_kernarg_segment_ptr();
  int b = blockIdx.x, tid = threadIdx.x;
  const int w = __builtin_amdgcn_readfirstlane(tid >> 6);
  STAMP(0);
  unsigned tag, gd0 = 0, gd1 = 0, pf = 0;
  if (CODE_BYTES && !(b & 8)) {
    unsigned long long code0; asm volatile("s_getpc_b64 %0" : "=s"(code0));
    if (tid >= 496) pf = __builtin_amdgcn_raw_buffer_load_b32(mk((const void*)code0, CODE_BYTES), (b >> 4) * 1024 + (tid - 496) * 64, 0, 0);
  }

  if (__builtin_expect(b & 8, 1)) {
    v4u aw[4], wv[8];
    v2u cv;
    unsigned ep;
    int va, vq;
    {
      u64x4 p4;
      asm volatile("s_load_dwordx8 %0, %1, 0x0" : "=&s"(p4) : "s"(ka));
      SB();
      const int r0 = 128 * (b & 7) + 16 * w + ((tid >> 2) & 12), cb = (tid & 15) * 16;
      int so = r0 * 2;
      va = r0 * 12288 + ((b >> 4) ^ CX) * 256 + cb;
      vq = (128 * (b & 6) + 32 * w + ((tid >> 4) & 3)) * 8192 + ((b & 1) * 16 + (b >> 4)) * 256 + cb;
      asm volatile("s_waitcnt lgkmcnt(0)" : "+v"(va), "+v"(so), "+v"(vq), "+s"(p4));
      SB();
      const rsrc_t rwm = mk((const void*)p4.y, D * 6144 * 2), rc = mk((const void*)p4.x, D * 2), rq = mk((const void*)p4.z, 8388608);
      ep = __builtin_amdgcn_raw_buffer_load_b32(mk((const void*)p4.w, w == 7 ? 8 : 0), WS_EPOCH + 4, 0, AUX_SC1);
      cv = __builtin_amdgcn_raw_buffer_load_b64(rc, so, 0, 0);
#pragma unroll
      for (int m = 0; m < 4; ++m) aw[m] = ldw(rwm, va, m * 12288);
      IBAR();
#pragma unroll
      for (int m = 0; m < BEARLY; ++m) wv[m] = ldw(rq, vq, m * 32768);
      SB();
    }
    asm volatile("" : "+s"(b), "+v"(tid), "+s"(ka));
    CTX();
    const v4u S0 = msk(dup16(silu(blo(cv.x))), dmask), S1 = msk(dup16(silu(bhi(cv.x))), dmask), S2 = msk(dup16(silu(blo(cv.y))), dmask), S3 = msk(dup16(silu(bhi(cv.y))), dmask);
    const int kb = P;
    int ard = L_RED1 + hf * 2048 + u5 * 16, adst = WS_H1 + (xcd * 16 + (j ^ CX)) * 512 + u5 * 16 + hf * 8;
    {
      int wr = L_RED1 + w * 512 + (2 * c16 + h) * 16;
      PIN(wr); PIN(ard); PIN(adst);
      f32x4 acc = mfma(S0, aw[0], f32x4{0, 0, 0, 0});
      acc = mfma(S1, aw[1], acc); acc = mfma(S2, aw[2], acc); acc = mfma(S3, aw[3], acc);
      if (lane < 32) *lp<f32x4>(L, wr) = acc;
      TAG_PUT();
      bar();
    }
    if (w == 0) final32(L, ard, 512, ws, adst, *lp<unsigned>(L, L_TAG));
    STAMP(10);
    IBAR();
    TAG_GET();
    {
      const rsrc_t rq = mk(ka->wq, 8388608);
      const v4u s0v = __builtin_amdgcn_raw_buffer_load_b128(rx, lane * 32, 0, 0), s1v = __builtin_amdgcn_raw_buffer_load_b128(rx, lane * 32 + 16, 0, 0);
      const unsigned xe = __builtin_amdgcn_raw_buffer_load_b16(rx, (256 * kb + 32 * w + 4 * c8 + h) * 2, 0, 0);
      const unsigned gcv = __builtin_amdgcn_raw_buffer_load_b32(rc, gr0 * 2, 0, 0);
#pragma unroll
      for (int m = BEARLY; m < 8; ++m) wv[m] = ldw(rq, vq, m * 32768);
      SB();
      float xv[16];
#pragma unroll
      for (int i = 0; i < 4; ++i) { xv[2 * i] = blo(s0v[i]); xv[2 * i + 1] = bhi(s0v[i]); xv[8 + 2 * i] = blo(s1v[i]); xv[9 + 2 * i] = bhi(s1v[i]); }
      float s = 0.f;
#pragma unroll
      for (int i = 0; i < 16; ++i) s += xv[i];
      const float mean = sum64(s) * (1.f / D);
      float q2 = 0.f;
#pragma unroll
      for (int i = 0; i < 16; ++i) { const float d = xv[i] - mean; q2 += d * d; }
      const float sdv = rbf(__builtin_sqrtf(rbf(rbf(sum64(q2) * (1.f / D)) + 1e-5f)));
      float nrm = rbf(rbf(blo(xe) - rbf(mean)) / sdv);
      gd0 = dup16(silu(blo(gcv))); gd1 = dup16(silu(bhi(gcv)));
      int wr = L_RED0 + w * 512 + (2 * c16 + h) * 16, rd = L_RED0 + hf * 2048 + u5 * 16, dst = WS_H2 + (pj * 4 + kb) * 512 + u5 * 16 + hf * 8;
      int off[2];
      off[0] = WS_H1 + (((lane >> 3) & 3) * 16 + hf * 8 + 2 * kb + (w >> 2)) * 512 + (w & 3) * 128 + c8 * 16; off[1] = off[0] + 32768;
      PIN(wr); PIN(rd); PIN(dst); PIN(nrm); PIN(off[0]); PIN(off[1]); PIN(gd0); PIN(gd1);
      asm volatile("s_waitcnt vmcnt(1)" ::: "memory");
      STAMP(1);
      v4u u[2]; gissue<2>(ws, off, u);
      unsigned xx;
      gfinish<2, false>(ws, off, u, tsh, [&]() {
        const F2 sc = red_pair(unpack4<true>(u[0]) + unpack4<true>(u[1]));
        const unsigned pk = cvtpk(sc.a, sc.b);
        xx = dup16(rbf(nrm * rbf(1.f + bhi(pk))) + blo(pk));
        PIN(xx);
      });
      STAMP(2);
      f32x4 acc = {0, 0, 0, 0};
#define GST(M) acc = mfma(diag<M>(xx, dmask), wv[M], acc);
      GST(0) GST(1) GST(2) GST(3) GST(4) GST(5) GST(6) GST(7)
#undef GST
      if (lane < 32) *lp<f32x4>(L, wr) = acc;
      bar();
      if (w == 0) { final32(L, rd, 512, ws, dst, tag); DRAIN(); }
      STAMP(3);
      IBAR();
    }
    const int cg = ef % 14, ks = ef / 14;
    const rsrc_t rg = mk(ka->wg, isR ? 0 : D * FF * 2), ru = mk(ka->wu, isR ? 0 : D * FF * 2);
    v4u ew[16];
    const int vo = (128 * ks + 16 * w + h) * (FF * 2) + cg * 512 + c16 * 16, vu = vo ^ 512;
    {
      v4u gw[2], sw[4];
      const int vg = gr0 * 12288 + 4096 + (j >> 1) * 256 + c16 * 16;
      gw[0] = ldw(rwm, vg, 0); gw[1] = ldw(rwm, vg, 12288);
#pragma unroll
      for (int m = 0; m < 4; ++m) sw[m] = ldw(rwm, va + 6144, m * 12288);
      IBAR();
      ew[0] = ldw(rg, vo, 0); ew[1] = ldw(ru, vu, 0); ew[2] = ldw(rg, vo + 256, 0); ew[3] = ldw(ru, vu + 256, 0);
      SB();
      f32x4 ag = mfma(msk(gd0, dmask), gw[0], f32x4{0, 0, 0, 0}), as = mfma(S0, sw[0], f32x4{0, 0, 0, 0});
      ag = mfma(msk(gd1, dmask), gw[1], ag);
      as = mfma(S1, sw[1], as); as = mfma(S2, sw[2], as); as = mfma(S3, sw[3], as);
      if (lane < 32) { *lp<f32x4>(L, L_RED2 + w * 1024 + (2 * c16 + h) * 16) = ag; *lp<f32x4>(L, L_RED2 + w * 1024 + 512 + (2 * c16 + h) * 16) = as; }
      bar();
      if (w < 2) final32(L, L_RED2 + hf * 4096 + w * 512 + u5 * 16, 1024, ws, (w ? WS_HSC + (xcd * 16 + (j ^ CX)) * 512 : WS_HGA + ((2 * xcd + (j & 1)) * 8 + (j >> 1)) * 512) + u5 * 16 + hf * 8, tag);
      STAMP(4);
      IBAR();
    }
    if (!isR) {
#pragma unroll
      for (int m = 1; m < 4; ++m) {
        ew[4 * m] = ldw(rg, vo, m * 4 * FF * 2); ew[4 * m + 1] = ldw(ru, vu, m * 4 * FF * 2);
        ew[4 * m + 2] = ldw(rg, vo + 256, m * 4 * FF * 2); ew[4 * m + 3] = ldw(ru, vu + 256, m * 4 * FF * 2);
      }
      SB();
      int wr = L_V + w * 2048 + (2 * c16 + h) * 16, rd = L_V + hf * 8192 + (w & 3) * 512 + u5 * 16;
      int dst = WS_H5 + (ks * 112 + ((cg ^ (w & 1)) * 2 + ((w >> 1) & 1)) * 4 + (u5 >> 3)) * 256 + (w & 1) * 128 + c8 * 16 + hf * 8;
      const int ln = 4 * ks + (w >> 1), un = ((w & 1) * 4 + (lane & 3)) * 16;
      int offa[2], offb[2];
      offa[0] = WS_HSC + ((lane >> 3) * 16 + (ln >> 2)) * 512 + (ln & 3) * 128 + un; offa[1] = offa[0] + 4096;
      offb[0] = (ALLSTAT || w == 0) ? WS_H4B + u5 * 256 + 128 : WS_H4B + ln * 256 + un; offb[1] = WS_H4B + ln * 256 + un;
      PIN(wr); PIN(rd); PIN(dst); PIN(offa[0]); PIN(offa[1]); PIN(offb[0]); PIN(offb[1]);
      asm volatile("s_waitcnt vmcnt(2)" ::: "memory");
      STAMP(6);
      v4u ua[2], ub[2];
      gissue<2>(ws, offa, ua); gissue<2>(ws, offb, ub);
      float sb, c1;
      if (__builtin_expect(!gok<2>(ua, tsh), 0)) { gretry<2, true>(ws, offa, ua, tsh); gissue<2>(ws, offb, ub); }
      sb = rbf(red8(unpack4<true>(ua[0]))); c1 = rbf(1.f + rbf(red8(unpack4<true>(ua[1]))));
      PIN(sb); PIN(c1);
      STAMP(7);
      float x1, mb, rs;
      gfinish<2, false>(ws, offb, ub, tsh, [&]() {
        const unsigned l0 = (h & 2) ? ub[1].z : ub[1].x, h0 = (h & 2) ? ub[1].w : ub[1].y;
        x1 = (h & 1) ? upb(l0) : upa<true>(l0, h0);
        const float mean = sum32(asf(ub[0].x)) * (1.f / D), ex2 = sum32(asf(ub[0].z)) * (1.f / D);
        mb = rbf(mean);
        rs = __builtin_amdgcn_rcpf(rbf(__builtin_amdgcn_sqrtf(rbf(rbf(__builtin_fmaxf(ex2 - mean * mean, 0.f)) + 1e-5f))));
        PIN(x1); PIN(mb); PIN(rs);
      });
      STAMP(8);
      if (!ALLSTAT) {
        if (tid == 0) *lp<f32x2>(L, L_ST) = f32x2{mb, rs};
        bar();
        const f32x2 stv = *lp<f32x2>(L, L_ST);
        mb = stv[0]; rs = stv[1];
      }
      const unsigned xx = dup16(rbf(rbf(rbf(x1 - mb) * rs) * c1) + sb);
      f32x4 ac[4] = {{0, 0, 0, 0}, {0, 0, 0, 0}, {0, 0, 0, 0}, {0, 0, 0, 0}};
#define EST(M) { const v4u A = diag<M>(xx, dmask); ac[0] = mfma(A, ew[4 * M], ac[0]); ac[1] = mfma(A, ew[4 * M + 1], ac[1]); ac[2] = mfma(A, ew[4 * M + 2], ac[2]); ac[3] = mfma(A, ew[4 * M + 3], ac[3]); }
      EST(0) EST(1) EST(2) EST(3)
#undef EST
      if (lane < 32) { *lp<f32x4>(L, wr) = ac[0]; *lp<f32x4>(L, wr + 512) = ac[1]; *lp<f32x4>(L, wr + 1024) = ac[2]; *lp<f32x4>(L, wr + 1536) = ac[3]; }
      bar();
      if (w < 4) final32(L, rd, 2048, ws, dst, tag);
      STAMP(9);
      FLUSH();
      return;
    }
  } else {
    asm volatile("" : "+s"(b), "+v"(tid), "+s"(ka));
    CTX();
    const int g = P, gd = (P + 1) & 3, wd = pj >> 2;
#define DST(M, W) { const v4u A = diag<M>(xx, dmask); acc = mfma(A, W[2 * M], acc); acd = mfma(A, W[2 * M + 1], acd); }
    {
      v4u vv[4], kk[4], kf[4], vf[4], dw[8];
      const int vkv = (g * SK + SK - WIN + pj * 128) * 256 + w * 4096 + lane * 16;
      const rsrc_t rk = mk(ka->kc, 67108864), rv = mk(ka->vc, 67108864), ro = mk(ka->wo, 4096 * D * 2);
      const int vod = ((gd * 8 + w) * 128 + 16 * wd + h) * 2048 + (pj & 3) * 512 + c16 * 16;
      if (X_DELAY) __builtin_amdgcn_s_sleep(X_DELAY);
      const unsigned ep = __builtin_amdgcn_raw_buffer_load_b32(mk(ka->ws, w == 7 ? 8 : 0), WS_EPOCH + 4, 0, AUX_SC1);
      const unsigned gcv = __builtin_amdgcn_raw_buffer_load_b32(rc, gr0 * 2, 0, 0);
#pragma unroll
      for (int t = 0; t < 4; ++t) vv[t] = ldw(rv, vkv, t * 1024);
#pragma unroll
      for (int t = 0; t < 4; ++t) kk[t] = ldw(rk, vkv, t * 1024);
      IBAR();
#pragma unroll
      for (int m = 0; m < 8; ++m) dw[m] = ldw(ro, vod + (m & 1) * 256, (m >> 1) * 8192);
      SB();
      if (lane < 8) lp<float>(L, L_WMAX)[lane] = -__builtin_inff();
      asm volatile("" :: "v"(pf));
      gd0 = dup16(silu(blo(gcv))); gd1 = dup16(silu(bhi(gcv)));
#pragma unroll
      for (int t = 0; t < 4; ++t) *lp<v4u>(L, L_V + (16 * w + 4 * t + h) * 256 + 16 * (c16 ^ ((h << 2) | t))) = vv[t];
#pragma unroll
      for (int t = 0; t < 4; ++t) *lp<v4u>(L, L_K + w * 4096 + (4 * t + h) * 256 + 16 * (c16 ^ (4 * t + h))) = kk[t];
      TAG_PUT();
      bar();
      TAG_GET();
      {
        const int q = c16 >> 2, p = c16 & 3, ch = 2 * w + (p >> 1);
        const unsigned base = (unsigned)(size_t)L + L_V + (8 * h + q) * 256 + 8 * (p & 1);
        const unsigned a0 = base + 16 * (ch ^ ((q << 2) | ((2 * h) & 3))), a1 = base + 1024 + 16 * (ch ^ ((q << 2) | ((2 * h + 1) & 3)));
#define VTR(S) { v2u lo, hi; asm volatile("ds_read_b64_tr_b16 %0, %2 offset:%4\n\tds_read_b64_tr_b16 %1, %3 offset:%4\n\ts_waitcnt lgkmcnt(0)" : "=&v"(lo), "=&v"(hi) : "v"(a0), "v"(a1), "i"(S * 8192) : "memory"); vf[S] = v4u{lo.x, lo.y, hi.x, hi.y}; }
        VTR(0) VTR(1) VTR(2) VTR(3)
#undef VTR
      }
#pragma unroll
      for (int s = 0; s < 4; ++s) kf[s] = *lp<v4u>(L, L_K + w * 4096 + c16 * 256 + 16 * ((4 * s + h) ^ c16));
      asm volatile("" : "+v"(kf[0]), "+v"(kf[1]), "+v"(kf[2]), "+v"(kf[3]));
      int dwr = L_RED2 + w * 1024 + (2 * c16 + h) * 16, drd = L_RED2 + hf * 4096 + (w & 1) * 512 + u5 * 16;
      int ddst = WS_H4A + (gd * 8 + wd) * 4096 + (pj & 3) * 1024 + (w & 1) * 512 + u5 * 16 + hf * 8;
      int doff[3];
      doff[0] = WS_H3D + ((gd * 32 + (lane >> 2)) * 32 + wd * 4 + (w >> 1)) * 128 + ((w & 1) * 4 + (lane & 3)) * 16; doff[1] = doff[0] + 65536;
      doff[2] = WS_H3S + (gd * 32 + 16 * (lane & 1) + (lane >> 2)) * 128 + (w >> 1) * 16;
      PIN(dwr); PIN(drd); PIN(ddst); PIN(doff[0]); PIN(doff[1]); PIN(doff[2]); PIN(gd0); PIN(gd1);
      {
        int qw = L_Q + w * 272 + u5 * 8 + hf * 4, fr = c8 * 272 + h * 16, mw = L_WMAX + c8 * 4, pw = L_P + c8 * 272 + w * 32 + h * 8;
        int od = WS_H3D + ((g * 32 + pj) * 32 + w * 4 + (c8 >> 1)) * 128 + ((c16 & 1) * 4 + h) * 16, sd = WS_H3S + (g * 32 + pj) * 128 + c8 * 8;
        int off[2] = {WS_H2 + ((g * 8 + w) * 4 + hf) * 512 + u5 * 16, WS_H2 + ((g * 8 + w) * 4 + 2 + hf) * 512 + u5 * 16};
        PIN(qw); PIN(fr); PIN(mw); PIN(pw); PIN(od); PIN(sd); PIN(off[0]); PIN(off[1]);
        asm volatile("s_waitcnt vmcnt(1)" ::: "memory");
        STAMP(3);
        v4u u[2]; gissue<2>(ws, off, u);
        unsigned qq;
        gfinish<2, false>(ws, off, u, tsh, [&]() {
          const f32x4 qs = unpack4<true>(u[0]) + unpack4<true>(u[1]);
          qq = cvtpk(tr32(qs[0], qs[2]), tr32(qs[1], qs[3]));
          PIN(qq);
        });
        STAMP(4);
        *lp<unsigned>(L, qw) = qq;
        bar();
        const v4u q0 = *lp<v4u>(L, L_Q + fr), q1 = *lp<v4u>(L, L_Q + fr + 64), q2 = *lp<v4u>(L, L_Q + fr + 128), q3 = *lp<v4u>(L, L_Q + fr + 192);
        SB();
        f32x4 sc = mfma(kf[0], q0, f32x4{0, 0, 0, 0});
        sc = mfma(kf[1], q1, sc); sc = mfma(kf[2], q2, sc); sc = mfma(kf[3], q3, sc);
        {
          const unsigned t0 = cvtpk(sc[0], sc[1]), t1 = cvtpk(sc[2], sc[3]);
          constexpr float SCL = 0.08838834764831845f;
          const unsigned r0 = cvtpk(blo(t0) * SCL, bhi(t0) * SCL), r1 = cvtpk(blo(t1) * SCL, bhi(t1) * SCL);
          sc = f32x4{blo(r0), bhi(r0), blo(r1), bhi(r1)};
        }
        const float mx = max3(max3(sc[0], sc[1], sc[2]), sc[3], sc[3]);
        if (c16 < 8) __hip_atomic_fetch_max(lp<float>(L, mw), mx, __ATOMIC_RELAXED, __HIP_MEMORY_SCOPE_WORKGROUP);
        bar();
        const float m = *lp<float>(L, mw);
        const float ml = m * LOG2E;
        const float p0 = __builtin_amdgcn_exp2f(sc[0] * LOG2E - ml), p1 = __builtin_amdgcn_exp2f(sc[1] * LOG2E - ml), p2 = __builtin_amdgcn_exp2f(sc[2] * LOG2E - ml), p3 = __builtin_amdgcn_exp2f(sc[3] * LOG2E - ml);
        if (c16 < 8) *lp<v2u>(L, pw) = v2u{cvtpk(p0, p1), cvtpk(p2, p3)};
        bar();
        const v4u b0 = *lp<v4u>(L, L_P + fr), b1 = *lp<v4u>(L, L_P + fr + 64), b2 = *lp<v4u>(L, L_P + fr + 128), b3 = *lp<v4u>(L, L_P + fr + 192);
        SB();
        f32x4 o = {0, 0, 0, 0};
        if (w == 0) {
          const v4u one = {0x3F803F80u, 0x3F803F80u, 0x3F803F80u, 0x3F803F80u};
          f32x4 l = mfma(one, b0, f32x4{0, 0, 0, 0}); o = mfma(vf[0], b0, o);
          l = mfma(one, b1, l); o = mfma(vf[1], b1, o); l = mfma(one, b2, l); o = mfma(vf[2], b2, o); l = mfma(one, b3, l); o = mfma(vf[3], b3, o);
          if (lane < 8) sth2(ws, sd, pack2<false>(l[0], m, tag));
        } else {
          o = mfma(vf[0], b0, o); o = mfma(vf[1], b1, o); o = mfma(vf[2], b2, o); o = mfma(vf[3], b3, o);
        }
        if (c16 < 8) sth(ws, od, pack4(o, tag));
        STAMP(5);
      }
      __builtin_amdgcn_s_sleep(H3WAIT);
      v4u u[3]; gissue<3>(ws, doff, u);
      unsigned xx;
      gfinish<3, false>(ws, doff, u, tsh, [&]() {
        const unsigned slo = (w & 1) ? u[2].z : u[2].x, shi = (w & 1) ? u[2].w : u[2].y;
        const float m = upb(slo), l = upa<false>(slo, shi);
        const float wg = __builtin_amdgcn_exp2f((m - maxb(m)) * LOG2E);
        float Lt = wg * l; Lt += dpp<XOR1>(Lt); Lt += dpp<ROR8>(Lt); Lt += dpp<ROR4>(Lt); Lt = tr16(Lt, Lt); Lt = tr32(Lt, Lt);
        const f32x4 v = unpack4<false>(u[0]) * dpp<QB0>(wg) + unpack4<false>(u[1]) * dpp<QB1>(wg);
        float o = red8(v); o += dpp<ROR4>(o);
        xx = dup16(o * __builtin_amdgcn_rcpf(Lt));
        PIN(xx);
      });
      STAMP(6);
      f32x4 acc = {0, 0, 0, 0}, acd = {0, 0, 0, 0};
      DST(0, dw) DST(1, dw) DST(2, dw) DST(3, dw)
      if (lane < 32) { *lp<f32x4>(L, dwr) = acc; *lp<f32x4>(L, dwr + 512) = acd; }
      bar();
      if (w < 2) final32(L, drd, 1024, ws, ddst, tag);
      STAMP(7);
    }
    if (!isR) {
      const unsigned tsh = tag << 12;
      __builtin_amdgcn_s_sleep(F_DELAY);
      IBAR();
      v4u gw[2], wv[8];
      const int vg = gr0 * 12288 + 10240 + (j >> 1) * 256 + c16 * 16;
      gw[0] = ldw(rwm, vg, 0); gw[1] = ldw(rwm, vg, 12288);
      IBAR();
      const rsrc_t rd = mk(ka->wd, FF * D * 2);
      const int vo = (128 * (ef >> 2) + 16 * w + h) * 2048 + (ef & 3) * 512 + c16 * 16;
#pragma unroll
      for (int m = 0; m < 8; ++m) wv[m] = ldw(rd, vo + (m & 1) * 256, (m >> 1) * 8192);
      SB();
      {
        f32x4 ag = mfma(msk(gd0, dmask), gw[0], f32x4{0, 0, 0, 0});
        ag = mfma(msk(gd1, dmask), gw[1], ag);
        if (lane < 32) *lp<f32x4>(L, L_RED1 + w * 512 + (2 * c16 + h) * 16) = ag;
        bar();
        if (w == 0) final32(L, L_RED1 + hf * 2048 + u5 * 16, 512, ws, WS_HGB + ((2 * xcd + (j & 1)) * 8 + (j >> 1)) * 512 + u5 * 16 + hf * 8, tag);
      }
      int wr = L_V + w * 1024 + (2 * c16 + h) * 16, rr = L_V + hf * 4096 + (w & 1) * 512 + u5 * 16;
      int dst = WS_H6 + (ef >> 2) * 4096 + (ef & 3) * 1024 + (w & 1) * 512 + u5 * 16 + hf * 8;
      int off[1] = {WS_H5 + (((lane >> 2) & 7) * 112 + (ef & ~3) + (w >> 1)) * 256 + hf * 128 + ((w & 1) * 4 + (lane & 3)) * 16};
      PIN(wr); PIN(rr); PIN(dst); PIN(off[0]);
      asm volatile("s_waitcnt vmcnt(1)" ::: "memory");
      STAMP(8);
      v4u u[1]; gissue<1>(ws, off, u);
      unsigned xx;
      gfinish<1, false>(ws, off, u, tsh, [&]() {
        F2 gu = red_pair(unpack4<false>(u[0]));
        gu.a += dpp<ROR4>(gu.a); gu.b += dpp<ROR4>(gu.b);
        xx = dup16(silu(gu.a) * gu.b);
        PIN(xx);
      });
      STAMP(9);
      f32x4 acc = {0, 0, 0, 0}, acd = {0, 0, 0, 0};
      DST(0, wv) DST(1, wv) DST(2, wv) DST(3, wv)
      if (lane < 32) { *lp<f32x4>(L, wr) = acc; *lp<f32x4>(L, wr + 512) = acd; }
      bar();
      if (w < 2) final32(L, rr, 1024, ws, dst, tag);
      STAMP(10);
      FLUSH();
      return;
    }
#undef DST
  }
  {
    const int lane = tid & 63, xcd = b & 7, j = b >> 4, c16 = lane & 15, h = lane >> 4, c8 = lane & 7, u5 = lane & 31, hf = lane >> 5;
    const int r = (xcd - 4) * 8 + ((b & 8) >> 1) + (j - 12);
    const rsrc_t ws = mk(ka->ws, WS_END), rout = mk(ka->out, D * 2);
    const unsigned tsh = tag << 12;
    float x1 = 0.f;
    const int lo = (lane >> 3) * 4096 + r * 128 + c8 * 16;
    auto gatevec = [&](int base) {
      int off[2] = {base + ((lane >> 3) * 8 + (r >> 2)) * 512 + (r & 3) * 128 + c8 * 16, 0};
      off[1] = off[0] + 32768;
      v4u u[2]; gissue<2>(ws, off, u);
      if (!gok<2>(u, tsh)) gretry<2, true>(ws, off, u, tsh);
      return rbf(red8(unpack4<true>(u[0]) + unpack4<true>(u[1])));
    };
    int oo = (32 * r + 4 * c8 + h) * 2;
    if (w == 0) {
      const unsigned xe = __builtin_amdgcn_raw_buffer_load_b16(mk(ka->x, D * 2), oo, 0, 0);
      float ga = gatevec(WS_HGA);
      int off[4] = {WS_H4A + lo, WS_H4A + lo + 32768, WS_H4A + lo + 65536, WS_H4A + lo + 98304};
      int dst = WS_H4B + r * 256 + ((lane & 8) ? 128 + c8 * 8 : c8 * 16 + hf * 8);
      float xf = blo(xe);
      PIN(off[0]); PIN(off[1]); PIN(off[2]); PIN(off[3]); PIN(dst); PIN(ga); PIN(xf); PIN(oo);
      if (H4WAIT) __builtin_amdgcn_s_sleep(H4WAIT);
      v4u u[4]; gissue<4>(ws, off, u);
      v2u gq;
      gfinish<4, false>(ws, off, u, tsh, [&]() {
        const float at = red8((unpack4<false>(u[0]) + unpack4<false>(u[1])) + (unpack4<false>(u[2]) + unpack4<false>(u[3])));
        x1 = rbf(xf + rbf(ga * rbf(at)));
        PIN(x1);
      });
      STAMP(11);
      float s1 = sum8(x1), s2 = sum8(x1 * x1);
      s1 = tr16(s1, s1); s2 = tr16(s2, s2);
      const float ss = tr32(s1, s1), sq = tr32(s2, s2);
      const F2 pr = sw16(x1, x1);
      gq = pack2<false>(pr.a, pr.b, tag);
      if (lane & 8) gq = v2u{asu(c8 ? sq : ss), tsh};
      if ((lane & 24) == 0 || lane == 8 || lane == 9) sth2(ws, dst, gq);
      if ((lane & 24) == 0) __builtin_amdgcn_raw_buffer_store_b32(0u, rout, oo, 0, 0);
      STAMP(12);
      DRAIN();
    }
    if (!(b & 8)) {
      IBAR();
      const rsrc_t rwm = mk(ka->wm, D * 6144 * 2);
      const int vg = (128 * xcd + 64 * (j & 1) + 8 * w + 2 * h) * 12288 + 10240 + (j >> 1) * 256 + c16 * 16;
      const v4u g0 = ldw(rwm, vg, 0), g1 = ldw(rwm, vg, 12288);
      const unsigned mv = c16 >= 8 ? 0u : (c16 & 1) ? 0xFFFF0000u : 0x0000FFFFu;
      const int t = c8 >> 1;
      const v4u dmask = {t == 0 ? mv : 0u, t == 1 ? mv : 0u, t == 2 ? mv : 0u, t == 3 ? mv : 0u};
      f32x4 ag = mfma(msk(gd0, dmask), g0, f32x4{0, 0, 0, 0});
      ag = mfma(msk(gd1, dmask), g1, ag);
      if (lane < 32) *lp<f32x4>(L, L_RED1 + w * 512 + (2 * c16 + h) * 16) = ag;
      bar();
      if (w == 0) final32(L, L_RED1 + hf * 2048 + u5 * 16, 512, ws, WS_HGB + ((2 * xcd + (j & 1)) * 8 + (j >> 1)) * 512 + u5 * 16 + hf * 8, tag);
    }
    if (w == 0) {
      float gb = gatevec(WS_HGB);
      int off[4] = {WS_H6 + lo, WS_H6 + lo + 32768, WS_H6 + lo + 65536, WS_H6 + (lo & ~0x4000) + 98304};
      float keep = lane < 32 ? 1.f : 0.f;
      PIN(off[0]); PIN(off[1]); PIN(off[2]); PIN(off[3]); PIN(gb); PIN(keep);
      v4u u[4]; gissue<4>(ws, off, u);
      unsigned res;
      gfinish<4, false>(ws, off, u, tsh, [&]() {
        const float y = red8((unpack4<false>(u[0]) + unpack4<false>(u[1])) + (unpack4<false>(u[2]) + unpack4<false>(u[3]) * keep));
        const float o = x1 + rbf(gb * rbf(y));
        const F2 pr = sw16(o, o);
        res = cvtpk(pr.a, pr.b);
        PIN(res);
      });
      STAMP(13);
      if ((lane & 24) == 0) __builtin_amdgcn_raw_buffer_store_b32(res, rout, oo, 0, 0);
      if (r == 0 && lane == 0) __builtin_amdgcn_raw_buffer_store_b32(tsh, ws, WS_EPOCH + 4, 0, AUX_SC1);
      STAMP(14);
    }
    FLUSH();
  }
}
}

extern "C" void kernel_launch(void* const* d_in, const int* in_sizes, int n_in, void* d_out, int out_size, void* d_ws, size_t ws_size, hipStream_t stream) {
  Args a;
  a.x = (const u16*)d_in[0]; a.kc = (const u16*)d_in[1]; a.vc = (const u16*)d_in[2]; a.wq = (const u16*)d_in[3]; a.wo = (const u16*)d_in[4];
  a.wg = (const u16*)d_in[6]; a.wu = (const u16*)d_in[7]; a.wd = (const u16*)d_in[8]; a.c = (const u16*)d_in[9]; a.wm = (const u16*)d_in[10];
  a.out = (u16*)d_out; a.ws = (unsigned char*)d_ws;
  hipLaunchKernelGGL(fused_block, dim3(256), dim3(512), 0, stream, a);
}
```
